```python
import torch

def _softmax(x, axis=-1):
    return torch.softmax(x, dim=axis)
def _gelu(x):
    return torch.nn.functional.gelu(x, approximate="tanh")
def _silu(x):
    return torch.nn.functional.silu(x)
def _sigmoid(x):
    return torch.sigmoid(x)
def _layernorm(x, axis=-1, eps=1e-5):
    m = x.mean(dim=axis, keepdim=True)
    v = x.var(dim=axis, keepdim=True, unbiased=False)
    return (x - m) / torch.sqrt(v + eps)
def _scatter_add(tgt, idx, src, axis=0):
    return tgt.clone().index_add_(axis, idx, src)
def _segment_sum(src, seg, n):
    out = torch.zeros((n,) + src.shape[1:], dtype=src.dtype)
    return out.index_add_(0, seg, src)
def _pad(x, pw):
    flat = tuple(v for pair in reversed(pw) for v in pair)
    return torch.nn.functional.pad(x, flat)

kernel_name = 'concat_heads__sliding_local_dilated__spectral_fnet__prompt'

def setup_inputs():

    _w = (lambda a: (lambda t: t.to(torch.bfloat16) if t.is_floating_point() else t)(torch.as_tensor(a)))
    def _A_setup():
        B, S, D = 1, 2048, 448
        return {
            "x": _w((lambda *s: torch.randn(*s, dtype=torch.bfloat16) * 0.2)(B, S, D)),
            "wqkv": _w((lambda *s: torch.randn(*s, dtype=torch.bfloat16) * 0.8)(D, 3 * D)),
            "wo": _w((lambda *s: torch.randn(*s, dtype=torch.bfloat16) * 0.2)(D, D)),
        }
    _A_in = _A_setup()

    def _B_setup():
        B, S, D = 1, 2048, 448
        ns = torch.arange(S)[:, None] * torch.arange(S)[None, :]
        nd = torch.arange(D)[:, None] * torch.arange(D)[None, :]
        Ws_re = torch.cos(-2.0 * torch.pi * ns / S).to(float)
        Ws_im = torch.sin(-2.0 * torch.pi * ns / S).to(float)
        Wd_re = torch.cos(-2.0 * torch.pi * nd / D).to(float)
        Wd_im = torch.sin(-2.0 * torch.pi * nd / D).to(float)
        return {
            "x": _w((lambda *s: torch.randn(*s, dtype=torch.bfloat16) * 0.05)(B, S, D)),
            "dft_seq_re": _w(Ws_re),
            "dft_seq_im": _w(Ws_im),
            "dft_hid_re": _w(Wd_re),
            "dft_hid_im": _w(Wd_im),
        }
    _B_in = _B_setup()

    return {
        "A_x": _A_in["x"],
        "A_wqkv": _A_in["wqkv"],
        "A_wo": _A_in["wo"],
        "B_x": _B_in["x"],
        "B_dft_seq_re": _B_in["dft_seq_re"],
        "B_dft_seq_im": _B_in["dft_seq_im"],
        "B_dft_hid_re": _B_in["dft_hid_re"],
        "B_dft_hid_im": _B_in["dft_hid_im"],
    }

def reference(A_x, A_wqkv, A_wo, B_x, B_dft_seq_re, B_dft_seq_im, B_dft_hid_re, B_dft_hid_im):
    def _A_ref(x, wqkv, wo):
        B, S, D = x.shape
        H = 16; Dh = D // H
        W, dil = 8, 2
        qkv = (lambda x, *d: x.permute(*d))((x @ wqkv).reshape(B, S, 3, H, Dh), 2, 0, 3, 1, 4)
        q, k, v = qkv[0], qkv[1], qkv[2]
        scale = Dh ** -0.5
        pos = torch.arange(S)
        off = torch.arange(W) * dil
        idx = pos[:, None] - off[None, :]
        mask = idx >= 0
        idx_c = torch.where(mask, idx, 0).reshape(-1)
        kg = k[:, :, idx_c, :].reshape(B, H, S, W, Dh)
        vg = v[:, :, idx_c, :].reshape(B, H, S, W, Dh)
        logits = torch.einsum("bhsd,bhswd->bhsw", q, kg) * scale
        logits = torch.where(mask[None, None, :, :], logits, -1e9)
        attn = _softmax(logits, axis=-1)
        o = torch.einsum("bhsw,bhswd->bhsd", attn, vg)
        return (lambda x, *d: x.permute(*d))(o, 0, 2, 1, 3).reshape(B, S, D) @ wo

    def _B_ref(x, dft_seq_re, dft_seq_im, dft_hid_re, dft_hid_im):
        a = torch.einsum("bsd,ed->bse", x, dft_hid_re)
        b = torch.einsum("bsd,ed->bse", x, dft_hid_im)
        y = torch.einsum("ts,bse->bte", dft_seq_re, a) - torch.einsum("ts,bse->bte", dft_seq_im, b)
        return y

    a = _A_ref(A_x, A_wqkv, A_wo)
    b = _B_ref(B_x, B_dft_seq_re, B_dft_seq_im, B_dft_hid_re, B_dft_hid_im)
    return torch.concatenate([a, b], axis=-1)
```

```cpp
#include <hip/hip_runtime.h>
#include <hip/hip_bf16.h>

typedef unsigned short u16;
typedef __attribute__((ext_vector_type(8))) short bf16x8;
typedef __attribute__((ext_vector_type(4))) short s16x4;
typedef __attribute__((ext_vector_type(4))) float f32x4;
typedef __attribute__((ext_vector_type(2))) __bf16 bf2;
typedef __attribute__((ext_vector_type(2))) unsigned u32x2;
typedef __attribute__((ext_vector_type(4))) unsigned u32x4;

#define LDS_AS __attribute__((address_space(3)))
#define GLB_AS __attribute__((address_space(1)))

constexpr int D = 448;

#define STAMP(kern, idx)
#define STAMPC(kern, idx)

typedef __attribute__((ext_vector_type(2))) float f32x2;
__device__ __forceinline__ unsigned pk2(float a, float b) {
    f32x2 v = {a, b};
    return __builtin_bit_cast(unsigned, __builtin_convertvector(v, bf2));
}
__device__ __forceinline__ void glds16a(const void* sbase, unsigned voff, const char* l) {
    const unsigned la = (unsigned)(size_t)(LDS_AS const char*)l;
    asm volatile("s_mov_b32 m0, %2\n\ts_nop 0\n\tglobal_load_lds_dwordx4 %0, %1" ::"v"(voff), "s"(sbase), "s"(la) : "memory");
}
__device__ __forceinline__ float vmax(float a, float b) { return __builtin_elementwise_maximum(a, b); }
__device__ __forceinline__ float vmax3(float a, float b, float c) { return vmax(vmax(a, b), c); }
__device__ __forceinline__ bf16x8 lds_rd128(const char* p) { return *reinterpret_cast<const bf16x8*>(p); }
__device__ __forceinline__ s16x4 lds_tr(const char* p) {
    return __builtin_amdgcn_ds_read_tr16_b64_v4i16((LDS_AS s16x4*)p);
}
__device__ __forceinline__ bf16x8 cat(s16x4 a, s16x4 b) {
    bf16x8 r;
    r[0] = a[0]; r[1] = a[1]; r[2] = a[2]; r[3] = a[3];
    r[4] = b[0]; r[5] = b[1]; r[6] = b[2]; r[7] = b[3];
    return r;
}
#define MFMA(a, b, c) __builtin_amdgcn_mfma_f32_16x16x32_bf16((a), (b), (c), 0, 0, 0)
template <int N> __device__ __forceinline__ void wait_vm() { asm volatile("s_waitcnt vmcnt(%0)" ::"i"(N) : "memory"); }
#define SB() __builtin_amdgcn_sched_barrier(0)
template <int NP, int NM> __device__ __forceinline__ void ilv() {
#pragma unroll
    for (int i = 0; i < NP; ++i) {
        __builtin_amdgcn_sched_group_barrier(0x8, 1, 0);
        __builtin_amdgcn_sched_group_barrier(0x100, 1, 0);
    }
    if constexpr (NM > 0) __builtin_amdgcn_sched_group_barrier(0x8, NM, 0);
}
#define BAR()                                               \
    do {                                                    \
        asm volatile("s_waitcnt lgkmcnt(0)" ::: "memory");  \
        __builtin_amdgcn_s_barrier();                       \
        asm volatile("" ::: "memory");                      \
    } while (0)
#define BARL()                                              \
    do {                                                    \
        asm volatile("" ::: "memory");                      \
        __builtin_amdgcn_s_barrier();                       \
        asm volatile("" ::: "memory");                      \
    } while (0)

#define BARK() do { __builtin_amdgcn_s_waitcnt(0xc07f); asm volatile("" ::: "memory"); __builtin_amdgcn_s_barrier(); asm volatile("" ::: "memory"); SB(); } while (0)
__device__ __forceinline__ int kperm(int r) { return (r & ~12) | ((r & 4) << 1) | ((r & 8) >> 1); }

constexpr int LDS_TOTAL = 163840;
constexpr int WS_O = 4 << 20, WS_PX = 8 << 20, WS_HS = 16 << 20;
#define CODEMAX 41984u
constexpr unsigned CODE_MAX = CODEMAX;

typedef unsigned long long u64;
struct Hs {
    const unsigned* base;
    unsigned off, snap;
};
constexpr int HS_O = 16 * 256, HS_X = 32 * 256;
constexpr u64 M_LO = 0xffffull, M_HI = 0xffff0000ull, M_OW = 0xfull << 48, M_OR = 1ull << 52, M_R14 = 0x3fffull << 16;
constexpr u64 M_XW = 0xfull << 32, M_XR = 0xf0ull << 32;
__device__ __forceinline__ unsigned hs_off(int lane, int b, int bx, int ro, int co) {
    const int bo = HS_O + ro * 256 - 48 * 4;
    return (unsigned)(lane * 4 + (lane < 32 ? b : lane < 48 ? bx - 32 * 4 : lane == 52 ? bo + 48 + co * 4 : bo));
}
template <int N> __device__ __forceinline__ unsigned hs_take(unsigned v) {
    unsigned r;
    asm volatile("s_waitcnt vmcnt(%2)\n\tv_mov_b32 %0, %1" : "=v"(r) : "v"(v), "i"(N) : "memory");
    return r;
}
__device__ __forceinline__ void hs_rmw(const Hs& h, unsigned& v, u64 m, unsigned d = 0u) {
    asm volatile("s_mov_b64 exec, %4\n\tglobal_atomic_add %0, %1, %2, %3 sc0 sc1\n\ts_mov_b64 exec, -1" : "+v"(v) : "v"(h.off), "v"(d), "s"(h.base), "s"(m) : "memory");
}
__device__ __forceinline__ void hs_add(const Hs& h, u64 m) {
    const unsigned one = 1u;
    asm volatile("s_mov_b64 exec, %3\n\tglobal_atomic_add %0, %1, %2 sc1\n\ts_mov_b64 exec, -1" ::"v"(h.off), "v"(one), "s"(h.base), "s"(m) : "memory");
}
__device__ __forceinline__ bool hs_new(const Hs& h, unsigned v, u64 m) { return (__ballot(v == h.snap) & m) == 0; }
#define PSL 2
__device__ __forceinline__ void hs_wait(const Hs& h, u64 m, u64 e, unsigned d) {
#pragma unroll 1
    for (unsigned spin = 0; spin < 40000u; ++spin) {
        unsigned v = 0;
        hs_rmw(h, v, e, d);
        if (hs_new(h, hs_take<0>(v), m)) return;
        __builtin_amdgcn_s_sleep(PSL);
    }
}
#define PD2 5
__device__ __forceinline__ void hs_wait2(const Hs& h, u64 m, u64 e, unsigned d, unsigned& a, unsigned& b) {
    unsigned n = 40000u;
    asm volatile("s_mov_b64 exec, %8\n"
                 "1:\n\tglobal_atomic_add %1, %3, %4, %5 sc0 sc1\n\ts_waitcnt vmcnt(1)\n\tv_cmp_eq_u32 vcc, %0, %6\n\ts_and_b64 vcc, vcc, %7\n\ts_cbranch_scc0 2f\n\t"
                 "global_atomic_add %0, %3, %4, %5 sc0 sc1\n\ts_waitcnt vmcnt(1)\n\tv_cmp_eq_u32 vcc, %1, %6\n\ts_and_b64 vcc, vcc, %7\n\ts_cbranch_scc0 2f\n\t"
                 "s_sub_u32 %2, %2, 1\n\ts_cbranch_scc0 1b\n"
                 "2:\n\ts_mov_b64 exec, -1"
                 : "+&v"(a), "+&v"(b), "+&s"(n) : "v"(h.off), "v"(d), "s"(h.base), "v"(h.snap), "s"(m), "s"(e) : "memory", "vcc", "scc");
}
__device__ __forceinline__ void hs_keep(unsigned a, unsigned b) { asm volatile("s_waitcnt vmcnt(0)" ::"v"(a), "v"(b) : "memory"); }
__device__ __forceinline__ bf16x8 ld16_p(__amdgpu_buffer_rsrc_t r, unsigned off) {
    return __builtin_bit_cast(bf16x8, __builtin_amdgcn_raw_buffer_load_b128(r, off, 0, 16));
}
__device__ __forceinline__ void st16_out(__amdgpu_buffer_rsrc_t r, unsigned off, u32x4 v) {
#define OPOL 16
    __builtin_amdgcn_raw_buffer_store_b128(v, r, off, 0, OPOL);
}
__device__ __forceinline__ void st16_sc1(__amdgpu_buffer_rsrc_t r, unsigned off, u32x4 v) {
    __builtin_amdgcn_raw_buffer_store_b128(v, r, off, 0, 16);
}
__device__ __forceinline__ void glds16m(const void* sbase, unsigned voff, const char* l, u64 mask) {
    const unsigned la = (unsigned)(size_t)(LDS_AS const char*)l;
    asm volatile("s_mov_b32 m0, %2\n\ts_mov_b64 exec, %3\n\tglobal_load_lds_dwordx4 %0, %1\n\ts_mov_b64 exec, -1" ::"v"(voff), "s"(sbase), "s"(la), "s"(mask) : "memory");
}
__device__ __forceinline__ void glds16c(const void* sbase, unsigned voff, const char* l) {
    const unsigned la = (unsigned)(size_t)(LDS_AS const char*)l;
    asm volatile("s_mov_b32 m0, %2\n\ts_nop 0\n\tglobal_load_lds_dwordx4 %0, %1 sc1" ::"v"(voff), "s"(sbase), "s"(la) : "memory");
}
__device__ __forceinline__ void glds16mc(const void* sbase, unsigned voff, const char* l, u64 mask) {
    const unsigned la = (unsigned)(size_t)(LDS_AS const char*)l;
    asm volatile("s_mov_b32 m0, %2\n\ts_mov_b64 exec, %3\n\tglobal_load_lds_dwordx4 %0, %1 sc1\n\ts_mov_b64 exec, -1" ::"v"(voff), "s"(sbase), "s"(la), "s"(mask) : "memory");
}
template <int SL> __device__ __forceinline__ void lds_wait(unsigned a) {
    unsigned n = 1000000u, v;
    asm volatile("1:\n\ts_sleep %3\n\tds_read_b32 %0, %2\n\ts_sub_u32 %1, %1, 1\n\ts_cbranch_scc1 2f\n\ts_waitcnt lgkmcnt(0)\n\tv_cmp_eq_u32 vcc, 0, %0\n\ts_cbranch_vccnz 1b\n"
                 "2:\n\ts_waitcnt lgkmcnt(0)" : "=&v"(v), "+s"(n) : "v"(a), "i"(SL) : "memory", "vcc", "scc");
}

constexpr int A_SL = 16384, B_R0 = 6 * A_SL, B_SL = 20480, X_SL = 8192, H_SL = 10240, H_0 = 11 * X_SL, L_KEY = H_0 + 7 * H_SL, L_GO = 163328, W_0 = A_SL;
constexpr int CODE_PART = (CODE_MAX - 6144) / 7 / 128 * 128, CODE_LN = (CODE_MAX / 128 + 23) / 24;
#define TT(i)
struct WO { const char* g; u64 pat; unsigned off, hi; };
__device__ __forceinline__ void wo_init(WO& q, int lane, int w) {
    q.g += w * (8 * 896);
    q.pat = 0ull;
#pragma unroll
    for (int i = 0; i < 21; ++i) q.pat |= (u64)(i % 7 == 6 ? 7 : (i % 7 + 1) / 2) << (3 * i);
    q.pat >>= 6 * w;
    q.hi = (unsigned)(lane >> 5) * (3 * 896);
    q.off = (unsigned)(((lane >> 5) * 16 + ((lane >> 3) & 3)) * 896 + ((lane & 7) ^ ((w & 1) * 4 + ((lane >> 4) & 1) * 2)) * 16);
    asm volatile("" : "+v"(q.off), "+v"(q.hi));
}
__device__ __forceinline__ void wo_req(WO& q, const char* l) {
    glds16a(q.g - ((unsigned)q.pat & 3) * (4 * 896), q.off + ((unsigned)q.pat & 4) * q.hi, l);
    q.g += 32 * 896; q.pat >>= 3;
}
#define SL1 1
#define SL2 1
#define N4 3
#define N5 4
#define N6 5
#define KA 3
#define V1 1
#define PDG 1
#define DEF 2
#define TC0 0x76542310u
#define TC1 0x76530421u
#define TC2 0x43176502u
#define LP 2
#define WP1 3
#define WP2 10
#define WE 3
struct BL {
    const char *xg, *hg;
    unsigned offX, offH[3];
    int kc;
};
__device__ __forceinline__ void reqX(BL& q, char* smem, int w, int sx) {
    const char* gx = q.xg + q.kc * 128;
    const char* la = smem + sx * X_SL + w * 2048;
    glds16a(gx, q.offX, la);
    glds16a(gx + 8 * 896, q.offX, la + 1024);
    q.kc = q.kc == 6 ? 0 : q.kc + 1;
}
__device__ __forceinline__ void reqH(BL& q, char* smem, int w, int k) {
    const char* gh = q.hg + q.kc * 128;
    const char* lh = smem + H_0 + k * H_SL + w * 2560;
    glds16a(gh, q.offH[0], lh);
    glds16a(gh, q.offH[1], lh + 1024);
    glds16m(gh, q.offH[2], lh + 2048, 0xffffffffull);
}
__device__ __forceinline__ void b_off(BL& q, int hn, int lane, int w) {
    const int r8 = lane >> 3, c = (lane & 7) ^ r8;
    q.offX = (unsigned)((w * 16 + r8) * 896 + c * 16);
#pragma unroll
    for (int i = 0; i < 3; ++i) q.offH[i] = (unsigned)(min(w * 20 + i * 8 + r8, hn) * 896 + (c ^ ((w & 1) * 4)) * 16);
}
__device__ __forceinline__ void b_load(char* smem, BL& q, const char* cg, const char* abg, u64 tb, int ks,
                                       bool narrow, const char* wog, bool duty, Hs& hs, int lane, int w, int pm, u64 pc) {
    const int r8 = lane >> 3, c = (lane & 7) ^ r8;
    unsigned offA[4];
#pragma unroll
    for (int i = 0; i < 4; ++i)
        offA[i] = (unsigned)(((w >> 1) * 64 + (((w & 1) * 2 + (i >> 1) + ks) & 3) * 16 + (i & 1) * 8 + r8) * 4096 + c * 16);
    const int rb0 = w * (narrow ? 32 : 40);
    unsigned offB = (unsigned)((c >> 2) * 10240 + (rb0 + r8) * 64 + (c & 3) * 16);
    u64 mB = narrow ? 1ull : ~0ull;
    int sa = 3;
    u64 tc = tb | 0x8888888800000000ull;
    const unsigned t3 = (unsigned)tb;
    auto reqC = [&]() {
        const char* g = cg + ((unsigned)tc & 15) * 128;
        const char* la = smem + sa * A_SL + w * 4096;
#pragma unroll
        for (int i = 0; i < 4; ++i) glds16a(g, offA[i], la + i * 1024);
        sa = sa == 5 ? 0 : sa + 1; tc >>= 4;
    };
    int sb = 0, lb0 = B_R0 + rb0 * 128;
    tb = tb << 1 | 0x1111111100000000ull;
    auto reqB = [&]() {
        const char* g = abg + ((unsigned)tb & 15) * B_SL;
        const char* lb = smem + lb0 + sb * B_SL;
#pragma unroll
        for (int i = 0; i < 4; ++i) glds16c(g + i * 512, offB, lb + i * 1024);
        glds16mc(g + 2048, offB, lb + 4096, mB);
        sb = sb == 2 ? 0 : sb + 1; tb >>= 4;
    };
    auto step = [&]() { reqB(); reqC(); wait_vm<13>(); BARL(); };
    u64 tt[11] = {0, 0, 0, 0, 0, 0, 0, 0, 0, 0, 0};
#pragma unroll 1
    for (int k = 2; k < 7; ++k) {
        reqH(q, smem, w, k); reqX(q, smem, w, k == 6 ? 10 : k);
        wait_vm<10>();
        BARL();
    }
    q.xg += 512 * 896;
    auto reqX2 = [&](int a, int b) {
#pragma unroll
        for (int i = a; i < b; ++i) reqX(q, smem, w, i == 6 ? 10 : i);
    };
    reqX2(0, N4);
    wait_vm<5 + 2 * N4>();
    BARL();
    reqX2(N4, N5);
    wait_vm<2 * N5>();
    BARL();
    TT(0);
    reqX2(N5, N6);
    unsigned own = (lane & 15) == pm, hv = 0, hw = 0;
    u64 m3 = 3u << (t3 << 1 & 14) | 3u << (t3 >> 3 & 14) | 3u << (t3 >> 7 & 14);
    asm volatile("" : "+v"(offA[0]), "+v"(offA[1]), "+v"(offA[2]), "+v"(offA[3]), "+v"(offB), "+v"(own));
    asm volatile("" : "+s"(mB), "+s"(m3), "+s"(abg), "+s"(cg), "+s"(lb0), "+s"(tb), "+s"(tc));
    if (w == 0) {
        const u32x2 t = *reinterpret_cast<const u32x2*>(smem + L_KEY + lane * 8);
        hs.off = t[0]; hs.snap = t[1];
    }
    wait_vm<2 * (N6 - KA)>();
    BARL();
    TT(1);
    reqX2(N6, 7);
    reqC();
    if (V1) reqC();
    wait_vm<4 + 4 * V1>();
    BARL();
    TT(2);
    if (w == 0) hs_add(hs, 1ull << pm);
    if (!V1) reqC();
#define PDA 5
    if (PDA && w == 0) { __builtin_amdgcn_s_sleep(PDA); hs_rmw(hs, hv, M_LO); }
    BARL();
    TT(3);
    if (w == 0) {
        if (!PDA) hs_rmw(hs, hv, M_LO);
        __builtin_amdgcn_s_sleep(PDG);
        hs_wait2(hs, m3, M_LO, own, hv, hw);
        *(volatile LDS_AS unsigned*)(smem + L_GO + 4) = 1u;
    } else lds_wait<SL1>(L_GO + 4);
    TT(4);
    reqB(); reqB();
    unsigned h2 = 0;
    if (w == 0) hs_rmw(hs, h2, M_LO);
    reqC();
    wait_vm<9>();
    asm volatile("" ::"v"(hv), "v"(hw));
    BARL();
#define CT 0
    auto code = [&]() {
        if (w == 2) asm volatile("s_mov_b32 m0, %2\n\ts_mov_b64 exec, %3\n\tglobal_load_lds_dword %0, %1\n\ts_mov_b64 exec, -1" ::"v"((unsigned)lane * 128u), "s"(pc), "i"(162816), "s"((1ull << CODE_LN) - 1) : "memory");
    };
    if (CT & 1) code();
    reqB(); reqC();
    {
        const unsigned v = hs_take<13>(h2);
        if (w == 0) { if (!hs_new(hs, v, M_LO)) hs_wait(hs, M_LO, M_LO, own); }
    }
    BARL();
    if (w == 0) hs_add(hs, 0x10001ull << pm | (duty ? M_OR : 0ull));
#pragma unroll 1
    for (int i = 1; i < 4; ++i) step();
    if (w == 0) hs_rmw(hs, h2, M_LO | M_HI, own);
    if (CT & 2) code();
    step();
    if (w == 0) { if (!hs_new(hs, hs_take<9>(h2), M_HI)) hs_wait(hs, M_HI, M_HI, 0u); }
#pragma unroll 1
    for (int i = 5; i < 13; ++i) step();
    WO wq;
    wq.g = wog;
    wo_init(wq, lane, w);
    reqB();
    if (duty) {
#pragma unroll
        for (int n = 0; n < 14; ++n) {
            if (n == WP1) { wait_vm<9 + WP1>(); BARL(); }
            if (n == WP2) { wait_vm<WP2>(); BARL(); }
            wo_req(wq, smem + W_0 + n * 4096 + w * 1024);
        }
    } else {
        wait_vm<9>(); BARL();
        wait_vm<0>(); BARL();
    }
    wait_vm<0>();
    BARL();
}
template <int NE, class F>
__device__ __forceinline__ void ab_mma(char* smem, unsigned dbase, unsigned sg, int lane, int sh, int ef, u16* ab, F post) {
    const int fr = lane & 15, fq = lane >> 4;
    const int z0 = fr * 128 + ((fq ^ (fr & 7)) * 16), z1 = z0 ^ 64;
    unsigned a0 = sh * 4096 + z0, a1 = sh * 4096 + z1, b0 = H_0 + ef * 2048 + z0, b1 = H_0 + ef * 2048 + z1;
    unsigned doff = dbase + (unsigned)(sh * 10240 + (ef * 16 + fr) * 64 + ((fq & 1) * 2 + (fq >> 1)) * 16);
    asm volatile("" : "+v"(a0), "+v"(a1), "+v"(b0), "+v"(b1), "+v"(doff));
    const char *sa0 = smem + a0, *sa1 = smem + a1, *sb0 = smem + b0, *sb1 = smem + b1;
    const __amdgpu_buffer_rsrc_t rs = __builtin_amdgcn_make_buffer_rsrc(ab, 0, 192 * B_SL, 0x00020000);
    f32x4 acc[NE][2];
    bf16x8 xf[2][2][2], hf[2][2][NE];
    auto rd = [&](int k, int i) {
        const int h = i >= 2 + NE, n = i - h * (2 + NE);
        if (n < 2) xf[k & 1][h][n] = lds_rd128((h ? sa1 : sa0) + (k == 6 ? 10 : k) * X_SL + n * 2048);
        else hf[k & 1][h][n - 2] = lds_rd128((h ? sb1 : sb0) + k * H_SL + (n - 2) * 2048);
    };
#pragma unroll 1
    for (int p = 0; p < 2; ++p) {
#pragma unroll
        for (int a = 0; a < NE; ++a) {
            acc[a][0] = f32x4{0.f, 0.f, 0.f, 0.f}; acc[a][1] = f32x4{0.f, 0.f, 0.f, 0.f};
            asm volatile("" : "+v"(acc[a][0]), "+v"(acc[a][1]));
        }
#pragma unroll
        for (int k = 0; k < 8; ++k) {
            if (k == 0) BARL();
            else if (k == KA) { if (p) wait_vm<0>(); BARL(); }
            else if (k == 7) { if (p) BAR(); }
            else if (!p) { if (k == 6) { post(); BAR(); } else BARL(); }
            if (k == 0 && p == 0) STAMP(0, 1);
            SB();
#pragma unroll
            for (int i = 0; i < 4 * NE; ++i) {
                const int h = i >= 2 * NE, n = i - h * 2 * NE;
                if (k == 7) acc[n >> 1][n & 1] = MFMA(xf[0][h][n & 1], hf[0][h][n >> 1], acc[n >> 1][n & 1]);
                else if (k) asm volatile("v_mfma_f32_16x16x32_bf16 %0, %1, %2, %0" : "+v"(acc[n >> 1][n & 1]) : "v"(xf[~k & 1][h][n & 1]), "v"(hf[~k & 1][h][n >> 1]));
                SB();
                if (k < 7 && i < 4 + 2 * NE) rd(k, i);
                SB();
            }
        }
#pragma unroll
        for (int mi = 0; mi < NE; ++mi) {
            const f32x4 a = acc[mi][0], b = acc[mi][1];
            auto x = __builtin_amdgcn_permlane16_swap(pk2(a[0], a[1]) ^ sg, pk2(b[0], b[1]) ^ sg, false, false);
            auto y = __builtin_amdgcn_permlane16_swap(pk2(a[2], a[3]) ^ sg, pk2(b[2], b[3]) ^ sg, false, false);
            st16_sc1(rs, doff + mi * 1024u, u32x4{x[0], y[0], x[1], y[1]});
        }
        SB();
        doff += B_SL;
    }
}
template <int NF>
__device__ __forceinline__ void y_mma(char* smem, int lane, int cw, u16* px, unsigned pxb, int ks, u16* out, unsigned ob0, Hs& hs) {
    const int fr = lane & 15, fq = lane >> 4;
    const int mh = cw >> 1, nh = cw & 1;
    f32x4 acc[5][4];
#pragma unroll
    for (int a = 0; a < NF; ++a)
#pragma unroll
        for (int b = 0; b < 4; ++b) { acc[a][b] = f32x4{0.f, 0.f, 0.f, 0.f}; asm volatile("" : "+v"(acc[a][b])); }
    const int z0 = fr * 128 + ((fq ^ (fr & 7)) * 16), z1 = z0 ^ 64;
    unsigned a0 = 3 * A_SL + mh * 8192 + z0, a1 = 3 * A_SL + mh * 8192 + z1, b0 = B_R0 + nh * (NF * 2048) + z0, b1 = B_R0 + nh * (NF * 2048) + z1;
    unsigned pl = pxb + (unsigned)(cw * 8192 + lane * 16), ph = pl + (unsigned)(6144 - 512 - lane * 8);
    if (NF == 5) asm volatile("" : "+v"(ph));
    unsigned ob = ob0 + (unsigned)((mh * 64 + fr) * 896 + nh * (NF * 16) + (fq & 1) * 16 + (fq >> 1) * 8) * 2u;
    const unsigned one = fq == (fr >> 2) ? 0x3f80u << ((fr & 1) * 16) : 0u, d0 = (fr & 2) ? 0u : one, d1 = (fr & 2) ? one : 0u;
    u32x4 s0 = {d0, d1, 0u, 0u}, s1 = {0u, 0u, d0, d1};
    unsigned own = lane == 32 + ks;
    asm volatile("" : "+v"(a0), "+v"(a1), "+v"(b0), "+v"(b1), "+v"(pl), "+v"(ob), "+v"(s0), "+v"(s1), "+v"(own));
    const char *sa0 = smem + a0, *sa1 = smem + a1, *sb0 = smem + b0, *sb1 = smem + b1;
    const __amdgpu_buffer_rsrc_t rs = __builtin_amdgcn_make_buffer_rsrc(px, 0, 48 * 4 * 32768, 0x00020000);
    const __amdgpu_buffer_rsrc_t rs_out = __builtin_amdgcn_make_buffer_rsrc(out, 0, 2048 * 896 * 2, 0x00020000);
    bf16x8 cf[2][4], xf[2][NF];
    auto rdx = [&](int mi, int k, int st) { xf[k][mi] = lds_rd128((k ? sb1 : sb0) + st * B_SL + mi * 2048); };
    auto rdc = [&](int l, int k, int st) { cf[k][l] = lds_rd128((k ? sa1 : sa0) + st * A_SL + l * 2048); };
    auto mf = [&](int mi, int l, int k) {
        asm volatile("v_mfma_f32_16x16x32_bf16 %0, %1, %2, %0" : "+v"(acc[mi][l]) : "v"(xf[k][mi]), "v"(cf[k][l]));
        SB();
    };
    int da = -3 * A_SL;
    auto head = [&](int st, int lo) {
        int n = 0;
        __builtin_amdgcn_s_waitcnt(0xc47f);
        SB();
#pragma unroll
        for (int i = 4 * lo; i < 16; ++i) {
            mf((i >> 1) & 1, i >> 2, i & 1);
            if (n < 2 * NF - 4) rdx(2 + (n >> 1), n & 1, st);
            if (st == 2 && n == 8) sa0 += da;
            if (st == 2 && n == 10) { sa1 += da; da = -da; }
            ++n;
            SB();
        }
        if (NF == 5) {
#pragma unroll
            for (int i = 2 * lo; i < 8; ++i) mf(2, i >> 1, i & 1);
        }
    };
    auto rest = [&](int st, int lo) {
#pragma unroll
        for (int i = 4 * lo; i < 16; ++i) {
            mf(NF - 2 + ((i >> 1) & 1), i >> 2, i & 1);
            if ((i & 3) >= 2) rdc(i >> 2, i & 1, st);
            else if (i - 4 * lo < 8) rdx((i >> 2) - lo, i & 1, st);
            else if (lo) rdc(0, i & 1, st);
            SB();
        }
    };
    STAMP(0, 4);
    BARL();
    STAMP(0, 5); STAMPC(0, 3);
#pragma unroll
    for (int i = 0; i < 16; ++i) {
        if ((i & 3) >= 2) rdc(i >> 2, i & 1, 0);
        else if (i < 8) rdx(i >> 2, i & 1, 0);
        SB();
    }
#pragma unroll 1
    for (int t = 0; t < 4; ++t) {
        head(0, 0);
        wait_vm<0>();
        BARK();
        rest(1, 0); head(1, 0);
        BARK();
        rest(2, 0); head(2, 0);
        BARK();
        rest(0, 0);
    }
    bf16x8 kf[4];
    unsigned ka = 0, kb = 0;
#define D3 0
    bf16x8 kx[2][NF], kc[2];
    head(0, D3); BARK();
    if (D3) {
#pragma unroll
        for (int i = 0; i < 2 * NF; ++i) kx[i & 1][i >> 1] = xf[i & 1][i >> 1];
        kc[0] = cf[0][0]; kc[1] = cf[1][0];
    }
    rest(1, D3); head(1, 1); BARK();
    kf[0] = cf[0][0]; kf[1] = cf[1][0];
    rest(2, 1); head(2, 1); BARK();
    hs_rmw(hs, kb, M_XR);
    kf[2] = cf[0][0]; kf[3] = cf[1][0];
    rest(0, 1);
    STAMP(0, 18); STAMPC(0, 4);
    const int pw = ks * 32768;
    auto put = [&](int p, int h) {
        const int sl = 2 * (p - 1) + h;
        const f32x4 a = acc[h < 2 ? 2 * h : NF - 1][p], b = acc[h < 2 ? 2 * h + 1 : 0][p];
        if (h < 2) __builtin_amdgcn_raw_buffer_store_b128(u32x4{pk2(a[0], a[1]), pk2(a[2], a[3]), pk2(b[0], b[1]), pk2(b[2], b[3])}, rs, pl + (sl & 3) * 1024u, pw + (sl >> 2 << 12), 16);
        else __builtin_amdgcn_raw_buffer_store_b64(u32x2{pk2(a[0], a[1]), pk2(a[2], a[3])}, rs, ph + p * 512u, pw, 16);
    };
    constexpr int NG = 2 * NF - 4, NL = 12 + 3 * NG;
#pragma unroll
    for (int i = 0; i < NL + 2 * NF; ++i) {
        const int j = i - 12, l = i < 12 ? 1 + (i >> 2) : i < NL ? 1 + j / NG : 0, mi = i < 12 ? (i >> 1) & 1 : i < NL ? 2 + j % NG / 2 : (i - NL) >> 1;
        acc[mi][l] = MFMA(xf[i & 1][mi], cf[i & 1][l], acc[mi][l]);
        SB();
        if (i < NG) rdx(2 + (i >> 1), i & 1, 0);
        const int e = i - 2;
        if (e == 4 || e == 8 || e == 12) put(e >> 2, 0);
        if (e > 12 && e <= NL && (e - 12) % NG == 4 % NG) put((e - 13) / NG + 1, 1);
        if (NF == 5 && e > 12 && e <= NL && (e - 12) % NG == 0) put((e - 12) / NG, 2);
        if (i > NL + 1) rdx((i - NL - 2) >> 1, i & 1, 1);
        if (i == NL + 3) BARL();
        SB();
    }
    rdx(NF - 1, 0, 1); rdx(NF - 1, 1, 1);
#pragma unroll
    for (int i = 0; i < 4 * NF; ++i) {
        const int mi = (i >> 1) % NF;
        acc[mi][0] = MFMA(xf[i & 1][mi], kf[(i >= 2 * NF) * 2 + (i & 1)], acc[mi][0]);
        SB();
        if (i < 2 * NF) rdx(mi, i & 1, 2);
        SB();
    }
#pragma unroll
    for (int mi = 0; mi < NF; ++mi) asm volatile("" : "+v"(acc[mi][0]));
    STAMP(0, 19);
    const bool all = hs_new(hs, hs_take<0>(kb), M_XR);
    STAMP(0, 20);
    hs_rmw(hs, ka, M_XW, own);
    if (D3) {
#pragma unroll
        for (int i = 0; i < 2 * NF; ++i) { acc[i >> 1][0] = MFMA(kx[i & 1][i >> 1], kc[i & 1], acc[i >> 1][0]); SB(); }
#pragma unroll
        for (int mi = 0; mi < NF; ++mi) asm volatile("" : "+v"(acc[mi][0]));
    }
    int pr[4];
#pragma unroll
    for (int l = 1; l < 4; ++l) pr[l - 1] = ((ks - l) & 3) * 32768 + (l == 3 ? 4096 : 0);
    pr[3] = pr[2] - 4096;
    unsigned pl2 = pl, ph2 = ph;
    u32x4 s01 = s0 | s1;
    __amdgpu_buffer_rsrc_t ro2 = rs_out;
    asm volatile("" : "+s"(pr[0]), "+s"(pr[1]), "+s"(pr[2]), "+s"(pr[3]), "+s"(ro2));
    asm volatile("" : "+v"(pl2), "+v"(ph2), "+v"(s01));
    auto fin = [&](int pr) {
        const f32x4 a = acc[2 * pr][0], b = acc[pr < 2 ? 2 * pr + 1 : 2 * pr][0];
        auto x = __builtin_amdgcn_permlane16_swap(pk2(a[0], a[1]), pk2(b[0], b[1]), false, false);
        auto y = __builtin_amdgcn_permlane16_swap(pk2(a[2], a[3]), pk2(b[2], b[3]), false, false);
        if (pr < 2 || !(fq & 1)) st16_out(ro2, ob + pr * 64u, u32x4{x[0], y[0], x[1], y[1]});
        SB();
    };
    __builtin_amdgcn_s_sleep(PD2 - 2 * D3);
    hs_wait2(hs, M_XW ^ 1ull << (32 + ks), M_XW ^ 1ull << (32 + ks), 0u, ka, kb);
    STAMP(1, 0);
    bf16x8 pv[2][3];
    u32x2 p5[3];
#pragma unroll
    for (int h = 0; h < (NF + 1) / 2; ++h)
#pragma unroll
        for (int l = 1; l < 4; ++l) {
            const int sl = 2 * (l - 1) + h;
            if (h < 2) pv[h][l - 1] = __builtin_bit_cast(bf16x8, __builtin_amdgcn_raw_buffer_load_b128(rs, pl2 + (sl & 3) * 1024u, pr[l - 1], 16));
            else p5[l - 1] = __builtin_amdgcn_raw_buffer_load_b64(rs, ph2 + l * 512u, pr[l == 3 ? 3 : l - 1], 16);
            SB();
        }
    if (!all) hs_add(hs, 1ull << (32 + ks));
#pragma unroll
    for (int h = 0; h < (NF + 1) / 2; ++h) {
        if (h < 2) {
#pragma unroll
            for (int l = 0; l < 3; ++l) {
                acc[2 * h][0] = MFMA(__builtin_bit_cast(bf16x8, s0), pv[h][l], acc[2 * h][0]);
                acc[2 * h + 1][0] = MFMA(__builtin_bit_cast(bf16x8, s1), pv[h][l], acc[2 * h + 1][0]);
                SB();
                if (h == 1 && l == 1) fin(0);
            }
        } else {
            acc[4][0] = MFMA(__builtin_bit_cast(bf16x8, s01), __builtin_bit_cast(bf16x8, u32x4{p5[0][0], p5[0][1], p5[1][0], p5[1][1]}), acc[4][0]);
            acc[4][0] = MFMA(__builtin_bit_cast(bf16x8, s0), __builtin_bit_cast(bf16x8, u32x4{p5[2][0], p5[2][1], 0u, 0u}), acc[4][0]);
            SB();
        }
    }
    STAMP(1, 1);
    fin(1);
    if (NF == 5) fin(2);
    hs_keep(ka, kb);
}

constexpr int Q_ASL = 18432, Q_WR = 3 * Q_ASL, Q_WSL = 21504, GP = 1216, Q_DUMMY = 162816, W_A = Q_DUMMY - 14 * 4096, Q_TAB = 84 * GP;
__device__ __forceinline__ void qkv_reqW(char* smem, const char* wg, unsigned off, int c, int kw, int sw) {
    const char* g = wg + (long)kw * (32 * 2688);
    const char* sb = smem + Q_WR + sw * Q_WSL + c * 1792;
#pragma unroll
    for (int k = 0; k < 6; ++k) glds16m(g + (k & 1) * (8 * 2688) + (k >> 1) * 896, off, sb + (k & 1) * 896 + (k >> 1) * 7168, 0xffffffffffffffull);
}
struct QX { unsigned off; int r0, r1, r2; };
__device__ __forceinline__ QX qkv_offX(int c, int lane, int s0) {
    QX q;
    q.off = (unsigned)((lane >> 3) * 1792 + ((lane & 7) ^ (lane >> 3)) * 16);
    q.r0 = max(s0 + (c < 2 ? c - 16 : 16 * c - 32), 0) * 896;
    q.r1 = (s0 + 16 * c + 32) * 896;
    q.r2 = q.r1 + (c < 2 ? 64 : -63) * 896;
    return q;
}
__device__ __forceinline__ void qkv_reqX(char* smem, const u16* __restrict__ xA, const QX& q, int c, int kx, int sx) {
    const char* g = (const char*)xA + kx * 128;
    const char* sb = smem + sx * Q_ASL + c * 1024;
    glds16a(g + q.r0, q.off, sb);
    glds16a(g + q.r1, q.off, sb + 4096);
    glds16a(g + q.r2, q.off, sb + 2 * 4096);
    glds16a(g + q.r1 + 896, q.off, sb + 3 * 4096);
    if (c < 2) glds16a(g + q.r1 + 65 * 896, q.off, sb + 4 * 4096);
}
template <int V> struct QS {
    static constexpr int col(int i) { constexpr int t[3][6] = {{0, 1, 2, 5, 3, 4}, {2, 3, 1, 0, 9, 10}, {0, 1, 2, 2, 3, 4}}; return t[V][i]; }
    static constexpr int wof(int i) { return V == 1 ? ((col(i) + 5) / 7) * 7168 + ((col(i) + 5) % 7 - 5) * 32 : col(i) * 32; }
    static constexpr int lo(int i) { return V == 0 ? (i == 3 ? 3 : 1) : V == 1 ? (i == 2 || i == 3 ? 1 : 0) : 0; }
    static constexpr int hi(int i) { return i != 3 || V == 0 ? 9 : V == 1 ? 3 : 0; }
};
#define RS2 0
#define LB 1
#define LC 3
#define LW 5
template <int V>
__device__ __forceinline__ void qkv_mma(char* smem, int lane, int G0, u64 (&tt)[11]) {
    typedef QS<V> S;
    const int fr = lane & 15, fq = lane >> 4;
    const int u = fq & 1, ah = fq >> 1, q = (lane >> 2) & 3, p = lane & 3;
    constexpr int R0 = V == 0 ? 1 : 0;
    f32x4 acc[6][9];
    unsigned aw = Q_WR + Q_WSL + (16 * ah + 4 * u + q) * 224 + 8 * p + (V < 2 ? G0 * 32 : (G0 >> 3) * 7168 + 64);
    unsigned ax0 = fr * 128 + ((fq ^ (fr & 7)) * 16), ax1 = ax0 ^ 64;
    unsigned ai = (G0 * 4 + fq) * GP + fr * 8, ah0 = ai + (fr & 8) * 64;
#pragma unroll
    for (int i = 0; i < 6; ++i)
#pragma unroll
        for (int r = S::lo(i); r < S::hi(i); ++r) { acc[i][r] = f32x4{0.f, 0.f, 0.f, 0.f}; asm volatile("" : "+v"(acc[i][r])); }
    asm volatile("" : "+v"(aw), "+v"(ax0), "+v"(ax1), "+v"(ai));
    if (V) asm volatile("" : "+v"(ah0));
    const char *pw = smem + aw, *px0 = smem + ax0, *px1 = smem + ax1;
    bf16x8 xf[9];
    s16x4 wl[6], wh[6];
    auto rdw = [&](int i, int h) {
        if (S::hi(i) == 0) return;
        if (h) wh[i] = lds_tr(pw + S::wof(i) + 8 * 224); else wl[i] = lds_tr(pw + S::wof(i));
    };
    int cw = 0x2108, cx = 0x24, dw = 0, dx = 0;
    auto tick = [&](int kind, bool extra) {
#pragma unroll
        for (int g = 0; g < 3; ++g) {
            if (g == 2 && (kind == 1 || extra)) BARK();
            int n = 0;
#pragma unroll
            for (int r = 0; r < 9; ++r)
#pragma unroll
                for (int e = 0; e < 2; ++e) {
                    const int i = 2 * g + e;
                    if (r < S::lo(i) || r >= S::hi(i)) continue;
                    asm volatile("v_mfma_f32_16x16x32_bf16 %0, %1, %2, %0" : "+v"(acc[i][r]) : "v"(cat(wl[i], wh[i])), "v"(xf[r]));
                    SB();
                    if (g < 2 ? n < 4 : (n < 8 && !(n & 1))) { const int m = g < 2 ? n : n >> 1; rdw(2 * ((g + 1) % 3) + (m >> 1), m & 1); }
                    if (g == 2 && e == 1) xf[r] = lds_rd128((kind ? px0 : px1) + r * 2048);
                    if (g == 0) {
                        if (n == 4) { dw = cw & 1 ? -4 * Q_WSL : Q_WSL; asm volatile("" : "+s"(dw)); }
                        if (n == 6) { cw >>= 1; asm volatile("" : "+s"(cw)); }
                        if (n == 8 && kind) { dx = cx & 1 ? -2 * Q_ASL : Q_ASL; asm volatile("" : "+s"(dx)); }
                        if (n == 10 && kind) { cx >>= 1; asm volatile("" : "+s"(cx)); px0 += dx; }
                        if (n == 12 && kind) px1 += dx;
                    }
                    if (g == 1 && n == 5) pw += dw;
                    ++n;
                    SB();
                }
        }
    };
    auto last = [&]() {
        int fi[48], fw[48], nf = 0, na = 99;
        u32x2 cv[48];
        auto behind = [&](int f) {
            const int a = f - LW, b = f - LC;
            if (a >= 0 && a < nf) {
                const int r = fw[a];
                *reinterpret_cast<u32x2*>(smem + (r ? ai : ah0) + S::col(fi[a]) * 4 * GP + (r == 0 ? 0 : r < 5 ? 128 * r - 64 : 128 * r)) = cv[a];
                SB();
            }
            if (b >= 0 && b < nf) { const f32x4 d = acc[fi[b]][fw[b]]; cv[b] = u32x2{pk2(d[0], d[1]), pk2(d[2], d[3])}; asm volatile("" : "+v"(cv[b])); }
            if (a == na + LB) { SB(); BARK(); }
        };
#pragma unroll
        for (int h = 0; h < (RS2 ? 6 : 3); ++h) {
            const int g = h % 3;
            int n = 0;
            if (h == 3) na = nf - 1;
#pragma unroll
            for (int r = 0; r < 9; ++r)
#pragma unroll
                for (int e = 0; e < 2; ++e) {
                    const int i = 2 * g + e;
                    if (r < S::lo(i) || r >= S::hi(i) || (RS2 && (0x67 >> r & 1) != (h < 3))) continue;
                    asm volatile("v_mfma_f32_16x16x32_bf16 %0, %1, %2, %0" : "+v"(acc[i][r]) : "v"(cat(wl[i], wh[i])), "v"(xf[r]));
                    SB();
                    if (n < 2 && h < 2) { rdw(2 * h + 2 + n, 0); rdw(2 * h + 2 + n, 1); }
                    fi[nf] = i; fw[nf] = r; ++nf;
                    behind(nf - 1);
                    ++n;
                    SB();
                }
        }
        asm volatile("s_nop 7");
#pragma unroll
        for (int f = 0; f < LW; ++f) { behind(nf + f); SB(); }
    };
    TT(9);
    __builtin_amdgcn_s_setprio(LP);
    BARL();
    TT(0); TT(5);
#pragma unroll
    for (int r = R0; r < 9; ++r) {
        if (r - R0 < 4) { rdw((r - R0) >> 1, (r - R0) & 1); SB(); }
        xf[r] = lds_rd128(px0 + r * 2048);
        SB();
    }
#pragma unroll 1
    for (int s = 0; s < 6; ++s) { tick(0, s == 0); tick(1, false); }
    TT(1); TT(6);
    tick(0, false);
    BARK();
    TT(2);
    last();
    TT(3); TT(7);
}

struct AMk { f32x4 c0, c1; u32x4 m; };
__device__ __forceinline__ AMk attn_mask(unsigned ln, bool top) {
    AMk t;
    const int out = (int)(~((0x1feu << (ln & 15)) & (top ? ~0xffu : ~0u)) >> (4 * (ln >> 4)));
    unsigned b0[4], b1[4];
#pragma unroll
    for (int i = 0; i < 4; ++i) {
        b0[i] = (unsigned)__builtin_amdgcn_sbfe(out, i, 1); b1[i] = (unsigned)__builtin_amdgcn_sbfe(out, 16 + i, 1);
        t.c0[i] = __uint_as_float(b0[i] & 0xf149f2cau);
        t.c1[i] = __uint_as_float(b1[i] & 0xf149f2cau);
    }
#pragma unroll
    for (int i = 0; i < 2; ++i) {
        t.m[i] = ~((b0[2 * i] & 0xffffu) | (b0[2 * i + 1] & 0xffff0000u));
        t.m[2 + i] = ~((b1[2 * i] & 0xffffu) | (b1[2 * i + 1] & 0xffff0000u));
    }
    asm volatile("" : "+v"(t.c0), "+v"(t.c1), "+v"(t.m));
    return t;
}
struct AA { u32x4 a; u32x2 b; };
__device__ __forceinline__ AA attn_addr(unsigned ln, int w, int rt, int hg) {
    AA t;
    const unsigned fr = ln & 15, fq = ln >> 4, b = (w >> 1) * 16, z = (w & 1) * 576, gq = ((fq & 1) * 2 + (fq >> 1)) * GP + z, gv = (56 + (ln & 3)) * GP + z;
    t.a[0] = gq + (b + fr) * 8;
    t.a[1] = fq == 3 ? 1152 - 64 : t.a[0];
    t.a[2] = gq + (b + 16 + min(fr, 7u)) * 8;
    t.a[3] = gv + (b + (ln >> 2)) * 8;
    t.b[0] = gv + (b + 16 + min(ln >> 2, 7u)) * 8;
    t.b[1] = (unsigned)(rt * 56 + hg * 14 + fq) * 2048u + (w * 16 + fr) * 16u;
    asm volatile("" : "+v"(t.a), "+v"(t.b));
    return t;
}
#define Q5 tp(0)
#define Q5N 1
#define Q6 tp(1)
#define Q7 tp(2); tp(3)
#define Q8 tw()
#define QD 0
__device__ __forceinline__ unsigned qkv_attn(char* smem, const u16* xA, const u16* wqkv, const char* wo, char* ws, int rt, int hg, int tid, int w,
                                         Hs& hs, int kst, int ro, int co, u64 cb, int qi, WO& wq) {
    const int lane = tid & 63, s0 = rt * 128, c = w & 3;
    auto st = [&](int i) { const int k = kst + i; return k >= 7 ? k - 7 : k; };
    u16* o = (u16*)(ws + WS_O);
    AMk am;
    AA aa;
    unsigned hv2 = 0;
    u64 tt[11] = {0, 0, 0, 0, 0, 0, 0, 0, 0, 0, 0};
    if (w >= 4) {
        if (w == 4) qkv_mma<0>(smem, lane, 0, tt);
        else if (w == 5) qkv_mma<1>(smem, lane, 5, tt);
        else qkv_mma<2>(smem, lane, 7 * w - 33, tt);
        __builtin_amdgcn_s_setprio(0);
        const char* tb = smem + W_A + c * 1024 + lane * 16;
        aa.a = *reinterpret_cast<const u32x4*>(tb); aa.b = *reinterpret_cast<const u32x2*>(tb + 4096);
        tb = smem + Q_TAB + lane * 16;
        am.c0 = *reinterpret_cast<const f32x4*>(tb); am.c1 = *reinterpret_cast<const f32x4*>(tb + 1024);
        am.m = *reinterpret_cast<const u32x4*>(tb + 2048);
        BAR();
        TT(4);
    } else {
#define LP0 1
        __builtin_amdgcn_s_setprio(LP0);
        const char* wg = (const char*)wqkv + ((c >> 1) * 16 + (c & 1) * 4) * 2688 + hg * 224;
        const unsigned offW = (unsigned)(lane * 16 + (lane * 37 >> 9) * (2688 - 224));
        if (QD) __builtin_amdgcn_s_sleep(QD);
        TT(0);
#define XF 0
        if (!XF) { qkv_reqW(smem, wg, offW, c, 2 * kst, 1); SB(); }
        const QX qx = qkv_offX(c, lane, s0);
        qkv_reqX(smem, xA, qx, c, kst, 0);
        if (XF) { SB(); qkv_reqW(smem, wg, offW, c, 2 * kst, 1); }
        TT(1);
        qkv_reqW(smem, wg, offW, c, 2 * kst + 1, 2);
        TT(2);
        wait_vm<6>();
        TT(4);
        BARL();
        __builtin_amdgcn_s_setprio(0);
        STAMP(0, 1);
        unsigned hv = 0;
        if (w == 0) { hs.off = hs_off(lane, HS_O + rt * 256, 0, ro, co); hs_rmw(hs, hv, M_R14 | M_OW); }
#define XO 0
        const bool late = XO == 1 || (XO == 2 && ((hg + 1) & 2));
        if (!late) qkv_reqX(smem, xA, qx, c, st(1), 1);
        qkv_reqW(smem, wg, offW, c, 2 * st(1), 3);
        if (late) wait_vm<6>(); else wait_vm<10>();
        BARL();
        STAMP(0, 9);
        int sa = 4, sx = late ? 1 : 2;
        const unsigned tc = (unsigned)((c * 32 + (lane >> 1)) * 4096), ts = (lane & 1) * 4;
        const unsigned tb = (0x76543210u + qi * 0x11111111u) & 0x77777777u | 0x88888888u;
        auto tp = [&](int i) { asm volatile("s_mov_b32 m0, %2\n\ts_nop 0\n\tglobal_load_lds_dword %0, %1" ::"v"(tc + ((tb >> 8 * i >> ts & 15) << 7)), "s"(cb), "i"(Q_DUMMY) : "memory"); };
#pragma unroll 1
        for (int s = 0; s < 5; ++s) {
            if (late) qkv_reqX(smem, xA, qx, c, st(s + 1), sx);
            qkv_reqW(smem, wg, offW, c, 2 * st(s + 1) + 1, sa);
            qkv_reqW(smem, wg, offW, c, 2 * st(s + 2), sa == 4 ? 0 : sa + 1);
            if (late) wait_vm<6>();
            else {
                qkv_reqX(smem, xA, qx, c, st(s + 2), sx);
                if (c < 2) wait_vm<11>(); else wait_vm<10>();
            }
            sa = sa >= 3 ? sa - 3 : sa + 2; sx = sx == 2 ? 0 : sx + 1;
            if (w == 0 && s == 3) { hs.snap = hs_take<63>(hv); hs_add(hs, M_OR); }
            BARL();
        }
        if (late) qkv_reqX(smem, xA, qx, c, st(6), 0);
        qkv_reqW(smem, wg, offW, c, 2 * st(6) + 1, 4);
        int l2 = lane;
        Q5;
        asm volatile("" : "+v"(l2));
        __builtin_assume(l2 >= 0 && l2 < 64);
        am = attn_mask(l2, rt + (w >> 1) == 0);
        aa = attn_addr(l2, w, rt, hg);
        const AA aa2 = attn_addr(l2, w + 4, rt, hg);
        unsigned wt = ((qi * 8 + w) * 49 + min(l2, 48)) * 128;
        asm volatile("" : "+v"(wt));
        auto tw = [&]() {
            asm volatile("s_mov_b32 m0, %2\n\ts_nop 0\n\tglobal_load_lds_dword %0, %1" ::"v"(wt), "s"(wo), "i"(Q_DUMMY) : "memory");
            asm volatile("s_mov_b32 m0, %2\n\ts_nop 0\n\tglobal_load_lds_dword %0, %1" ::"v"(wt), "s"(wo + 4 * 49 * 128), "i"(Q_DUMMY) : "memory");
        };
        wait_vm<Q5N>();
        BARL();
        Q6;
        {
            char* tq = smem + W_A + c * 1024 + l2 * 16;
            *reinterpret_cast<u32x4*>(tq) = aa2.a; *reinterpret_cast<u32x2*>(tq + 4096) = aa2.b;
            if (w == 3) {
                tq = smem + Q_TAB + l2 * 16;
                *reinterpret_cast<f32x4*>(tq) = am.c0; *reinterpret_cast<f32x4*>(tq + 1024) = am.c1; *reinterpret_cast<u32x4*>(tq + 2048) = am.m;
            }
        }
        BAR();
        if (w == 3) *reinterpret_cast<u32x2*>(smem + ((l2 & 3) * 7 + 4) * GP + 1152) = u32x2{0u, 0u};
        Q7;
        if (w == 0) hs_rmw(hs, hv2, M_R14);
        TT(3);
        BAR();
        Q8;
    }
    const unsigned m0[2] = {am.m[0], am.m[1]}, m1[2] = {am.m[2], am.m[3]};
    const f32x4 c0 = am.c0, c1 = am.c1;
    const float scale = 0.18898223650461361f, log2e = 1.4426950408889634f;
#define FTA(i)
    FTA(0);
    auto rd64 = [&](unsigned a, int g) { return *reinterpret_cast<const s16x4*>(smem + a + g * GP); };
    bf16x8 qf[4], k0[4], k1[4], v0[4], v1[4];
#pragma unroll
    for (int hh = 0; hh < 4; ++hh) {
        qf[hh] = cat(rd64(aa.a[0] + 64, 7 * hh), rd64(aa.a[1] + 64, 7 * hh + 4));
        k0[hh] = cat(rd64(aa.a[0], 28 + 7 * hh), rd64(aa.a[0], 32 + 7 * hh));
        k1[hh] = cat(rd64(aa.a[2], 28 + 7 * hh), rd64(aa.a[2], 32 + 7 * hh));
        SB();
    }
#pragma unroll
    for (int hh = 0; hh < 4; ++hh) {
        v0[hh] = cat(lds_tr(smem + aa.a[3] + 7 * hh * GP), lds_tr(smem + aa.b[0] + 7 * hh * GP));
        v1[hh] = cat(lds_tr(smem + aa.a[3] + (7 * hh + 4) * GP), lds_tr(smem + aa.b[0] + (7 * hh + 4) * GP));
    }
    SB();
    FTA(1);
#define AB2 1
    if (RS2 && AB2 == 0 && w < 4) { SB(); BARL(); SB(); }
    float l[2][4], mx[2];
#pragma unroll
    for (int hp = 0; hp < 2; ++hp) {
        const f32x4 a0 = MFMA(k0[2 * hp], qf[2 * hp], c0), a1 = MFMA(k1[2 * hp], qf[2 * hp], c1);
        const f32x4 b0 = MFMA(k0[2 * hp + 1], qf[2 * hp + 1], c0), b1 = MFMA(k1[2 * hp + 1], qf[2 * hp + 1], c1);
        float t[4];
#pragma unroll
        for (int i = 0; i < 4; ++i) {
            auto r = __builtin_amdgcn_permlane32_swap(__float_as_uint(vmax(a0[i], a1[i])), __float_as_uint(vmax(b0[i], b1[i])), false, false);
            t[i] = vmax(__uint_as_float(r[0]), __uint_as_float(r[1]));
        }
#pragma unroll
        for (int i = 0; i < 2; ++i) {
            unsigned a = pk2(t[2 * i], t[2 * i + 1]);
            a = pk2(__uint_as_float(a << 16) * scale, __uint_as_float(a & 0xffff0000u) * scale);
            l[hp][2 * i] = __uint_as_float(a << 16);
            l[hp][2 * i + 1] = __uint_as_float(a & 0xffff0000u);
        }
        mx[hp] = vmax(vmax3(l[hp][0], l[hp][1], l[hp][2]), l[hp][3]);
    }
    {
        auto r = __builtin_amdgcn_permlane16_swap(__float_as_uint(mx[0]), __float_as_uint(mx[1]), false, false);
        const float c = vmax(__uint_as_float(r[0]), __uint_as_float(r[1])) * -log2e;
        auto q2 = __builtin_amdgcn_permlane16_swap(__float_as_uint(c), __float_as_uint(c), false, false);
        mx[0] = __uint_as_float(q2[0]); mx[1] = __uint_as_float(q2[1]);
    }
    if (RS2 && AB2 == 1 && w < 4) { SB(); BARL(); SB(); }
    float sm[2];
#pragma unroll
    for (int hp = 0; hp < 2; ++hp) {
#pragma unroll
        for (int i = 0; i < 4; ++i) l[hp][i] = __builtin_amdgcn_exp2f(__builtin_fmaf(l[hp][i], log2e, mx[hp]));
        sm[hp] = (l[hp][0] + l[hp][1]) + (l[hp][2] + l[hp][3]);
    }
    {
        auto r = __builtin_amdgcn_permlane16_swap(__float_as_uint(sm[0]), __float_as_uint(sm[1]), false, false);
        const float c = __builtin_amdgcn_rcpf(__uint_as_float(r[0]) + __uint_as_float(r[1]));
        auto q2 = __builtin_amdgcn_permlane16_swap(__float_as_uint(c), __float_as_uint(c), false, false);
        sm[0] = __uint_as_float(q2[0]); sm[1] = __uint_as_float(q2[1]);
    }
    FTA(2);
    const f32x4 z = {0.f, 0.f, 0.f, 0.f};
    const __amdgpu_buffer_rsrc_t rs_o = __builtin_amdgcn_make_buffer_rsrc(o, 0, 2048 * 448 * 2, 0x00020000);
    const bool top = aa.a[0] != aa.a[1];
#pragma unroll
    for (int hp = 0; hp < 2; ++hp) {
        const unsigned p01 = pk2(l[hp][0] * sm[hp], l[hp][1] * sm[hp]), p23 = pk2(l[hp][2] * sm[hp], l[hp][3] * sm[hp]);
        auto e01 = __builtin_amdgcn_permlane32_swap(p01, p01, false, false);
        auto e23 = __builtin_amdgcn_permlane32_swap(p23, p23, false, false);
        u32x4 ov[2];
#pragma unroll
        for (int e = 0; e < 2; ++e) {
            const int hh = 2 * hp + e;
            u32x4 pw = {e01[e] & m0[0], e23[e] & m0[1], e01[e] & m1[0], e23[e] & m1[1]};
            const bf16x8 pf = __builtin_bit_cast(bf16x8, pw);
            const f32x4 o0 = MFMA(v0[hh], pf, z), o1 = MFMA(v1[hh], pf, z);
            ov[e] = u32x4{pk2(o0[0], o0[1]), pk2(o0[2], o0[3]), pk2(o1[0], o1[1]), pk2(o1[2], o1[3])};
        }
        ov[0][2] = top ? ov[1][0] : ov[0][2]; ov[0][3] = top ? ov[1][1] : ov[0][3];
        __builtin_amdgcn_raw_buffer_store_b128(ov[0], rs_o, aa.b[1], hp * 7 * 2048, 16);
        if (!top) __builtin_amdgcn_raw_buffer_store_b128(ov[1], rs_o, aa.b[1], (hp * 7 + 4) * 2048, 16);
    }
    FTA(4);
    if (w >= 4) {
        int l2 = lane;
        asm volatile("" : "+v"(l2));
        __builtin_assume(l2 >= 0 && l2 < 64);
        wo_init(wq, l2, c);
#pragma unroll 1
        for (int t = 0; t < WE; ++t) wo_req(wq, smem + W_A + t * 4096 + c * 1024);
    } else wait_vm<0>();
    const unsigned hv3 = hs_take<WE>(hv2);
    FTA(5);
    BARL();
    return hv3;
}
__device__ __forceinline__ void oproj(char* smem, u16* __restrict__ o, u16* __restrict__ out, int rt, int rg, int ct, int lane) {
    const int fr = lane & 15, fq = lane >> 4;
    const __amdgpu_buffer_rsrc_t rs_o = __builtin_amdgcn_make_buffer_rsrc(o, 0, 2048 * 448 * 2, 0x00020000);
    const unsigned ga = (unsigned)(rt * 56 + fq) * 2048u + (unsigned)(rg * 16 + fr) * 16u;
    bf16x8 fa[14];
#pragma unroll
    for (int ks = 0; ks < 14; ++ks) fa[ks] = ld16_p(rs_o, ga + ks * 8192u);
    SB();
    const int q = (lane >> 2) & 3, p = lane & 3, s = 2 * (fq & 1) + (q >> 1);
    const char* aw[4];
#pragma unroll
    for (int c = 0; c < 4; ++c) aw[c] = smem + (8 * fq + q) * 128 + ((c ^ s) * 32) + 8 * p;
    f32x4 acc[4];
#pragma unroll
    for (int c = 0; c < 4; ++c) acc[c] = f32x4{0.f, 0.f, 0.f, 0.f};
    constexpr int OA = 9;
    bf16x8 fb[14][4];
#pragma unroll
    for (int ks = 0; ks < 14 + OA; ++ks) {
        if (ks >= OA) {
#pragma unroll
            for (int c = 0; c < 4; ++c) acc[c] = MFMA(fb[ks - OA][c], fa[ks - OA], acc[c]);
        }
        if (ks < 14) {
#pragma unroll
            for (int c = 0; c < 4; ++c) fb[ks][c] = cat(lds_tr(aw[c] + ks * 4096), lds_tr(aw[c] + ks * 4096 + 512));
        }
        SB();
    }
    STAMP(1, 3);
    const __amdgpu_buffer_rsrc_t rs_out = __builtin_amdgcn_make_buffer_rsrc(out, 0, 2048 * 896 * 2, 0x00020000);
    const unsigned ob = (unsigned)((rt * 128 + (rg >> 1) * 32 + 2 * fr + (rg & 1)) * 896 + ct * 64 + (fq & 1) * 16 + (fq >> 1) * 8) * 2u;
#pragma unroll
    for (int pr = 0; pr < 2; ++pr) {
        const f32x4 a = acc[2 * pr], b = acc[2 * pr + 1];
        auto x = __builtin_amdgcn_permlane16_swap(pk2(a[0], a[1]), pk2(b[0], b[1]), false, false);
        auto y = __builtin_amdgcn_permlane16_swap(pk2(a[2], a[3]), pk2(b[2], b[3]), false, false);
        st16_out(rs_out, ob + pr * 64u, u32x4{x[0], y[0], x[1], y[1]});
    }
}

__global__ __launch_bounds__(512) void kf(const u16* xA, const u16* wqkv, const char* xB, const char* Hr, const char* Hi, const char* Cr,
                                          const char* Ci, const char* wo, u16* out, char* ws) {
    __shared__ __attribute__((aligned(16))) char smem[LDS_TOTAL];
    int tid = threadIdx.x, bid = blockIdx.x;
    u64 pc0;
    {
        unsigned part, off;
        asm volatile("s_getpc_b64 %0\n\ts_bfe_u32 %1, %2, 0x30003\n\ts_mulk_i32 %1, %3" : "=&s"(pc0), "=&s"(part), "+s"(bid) : "i"(CODE_PART) : "scc");
        asm volatile("s_mov_b32 m0, %4\n\tv_mad_u32_u24 %0, %1, 12, %2\n\tglobal_load_lds_dword %0, %3" : "=&v"(off), "+v"(tid) : "s"(part), "s"(pc0), "i"(Q_DUMMY) : "memory");
        asm volatile("" : "+s"(bid));
    }
    __builtin_assume(tid >= 0 && tid < 512 && bid >= 0 && bid < 256);
    const int lane = tid & 63, xcd = bid & 7;
    int w = __builtin_amdgcn_readfirstlane(tid >> 6), j = bid >> 3;
    STAMP(0, 0);
    const int ks = xcd & 3, rl = j & 3, c6 = j >> 2, r8 = (xcd >> 2) * 4 + rl;
    BL q;
    if (j < 24 && w < 4) {
        unsigned ox = (unsigned)(((ks & 1) * 1024 + r8 * 64) * 896), oh = (unsigned)((c6 < 5 ? c6 * 80 : 384) * 896);
#define KR 1
        q.kc = c6 + KR * rl >= 7 ? c6 + KR * rl - 7 : c6 + KR * rl;
        b_off(q, c6 < 4 ? 79 : 63, lane, w);
        const char *p0 = xB, *p1 = Hr, *p2 = Hi;
        asm volatile("" : "+v"(q.offX), "+v"(q.offH[0]), "+v"(q.offH[1]), "+v"(q.offH[2]));
        asm volatile("" : "+s"(p0), "+s"(p1), "+s"(p2), "+s"(ox), "+s"(oh), "+s"(q.kc));
        q.xg = p0 + ox;
        q.hg = ((ks >> 1) ? p2 : p1) + oh;
        STAMP(0, 7);
        reqH(q, smem, w, 0); reqX(q, smem, w, 0);
        reqH(q, smem, w, 1); reqX(q, smem, w, 1);
        asm volatile("" ::"s"(xA), "s"(wqkv), "s"(Cr), "s"(Ci), "s"(wo), "s"(out), "s"(ws));
    }
    asm volatile("" : "+s"(w), "+s"(j));
    __builtin_assume(w >= 0 && w < 8 && j >= 0 && j < 32);
    if (w == 7) *reinterpret_cast<u32x2*>(smem + L_GO) = u32x2{0u, 0u};
    Hs hs;
    hs.base = (const unsigned*)(ws + WS_HS); hs.off = 0; hs.snap = 0;
    int ro = 0, rg = 0, ct = 0;
    bool duty = true;
    u64 again = 0, mr = M_R14;
    unsigned ka = 0, kb = 0;
    int wb = W_0;
    if (j < 24) {
        u16* ab = (u16*)ws;
        u16* px = (u16*)(ws + WS_PX);
        const int rbl = j & 7, cg = j >> 3, rb = (xcd >> 2) * 8 + rbl;
#define DU 1
#define U2 0
        duty = DU ? (j & 19) != 3 : j >= 4;
        const int un = DU ? j - min(c6, 4) : j - 4, uc = un >= 14 ? un - 14 : un;
        ro = xcd * 2 + (un >= 14); ct = (uc >> 1) & 7; rg = (uc & 1) * 4 + (w & 3);
        const int g1 = ks * 3 + cg;
        if (w < 4) {
            const char* cgp = ((ks >> 1) ? Ci : Cr) + (long)rb * (128 * 4096) + (ks & 1) * 2048;
            b_load(smem, q, cgp, (const char*)ab + g1 * (16 * B_SL), (u64)((0x76543210u + rbl * 0x11111111u) & 0x77777777u) << 32 |
                   (((cg == 0 ? TC0 : cg == 1 ? TC1 : TC2) + rbl * 0x11111111u) & 0x77777777u), ks, cg == 2,
                   wo + (duty ? ct : 0) * 128, duty, hs, lane, w, r8 * 2 + (c6 & 1),
                   pc0 + (unsigned)min(j * CODE_LN, (int)CODE_MAX / 128 - CODE_LN) * 128u);
        } else {
            const int cw = w - 4;
            asm volatile("" ::"s"(xB), "s"(Hr), "s"(Hi));
            hs.off = hs_off(lane, g1 * 256, HS_X + ((rb * 3 + cg) * 4 + cw) * 256, ro, uc & 15);
            unsigned v1 = 0;
            hs_rmw(hs, v1, w == 5 ? M_LO | M_HI | M_XW | M_XR | M_OW : M_XW | M_XR);
            STAMP(0, 7);
            auto post = [&]() {
                if (w == 5) *reinterpret_cast<u32x2*>(smem + L_KEY + lane * 8) = u32x2{hs.off, hs_take<0>(v1)};
            };
            const unsigned dbase = (unsigned)((g1 * 16 + r8 * 2) * B_SL + (c6 & 1) * (c6 < 4 ? 80 : 64) * 64);
            const unsigned sg = (ks >> 1) ? 0x80008000u : 0u;
            if (c6 < 4 && !(cw & 1)) ab_mma<3>(smem, dbase, sg, lane, cw >> 1, 0, ab, post);
            else ab_mma<2>(smem, dbase, sg, lane, cw >> 1, (cw & 1) * (c6 < 4 ? 3 : 2), ab, post);
            hs.snap = hs_take<63>(v1);
            hs_add(hs, 1ull << (36 + ks));
            const unsigned pxb = (unsigned)(rb * 3 + cg) * 131072u, ob0 = (unsigned)((rb * 128 + ks * 16) * 896 + 448 + cg * 160) * 2u;
            if (cg == 2) y_mma<4>(smem, lane, cw, px, pxb, ks, out, ob0, hs);
            else y_mma<5>(smem, lane, cw, px, pxb, ks, out, ob0, hs);
            duty = false;
        }
    } else {
        const int qi = j - 24;
#define AM 1
#define HGM 0x3102
        const int hg = HGM >> (4 * ks) & 3, rt = (xcd >> 2) * 8 + qi;
#define XT4 0x5401
        int kst = qi + (XT4 >> (4 * hg) & 15); kst = kst >= 7 ? kst - 7 : kst;
        ro = xcd * 2 + 1; ct = 3 + (qi >> 1); rg = (qi & 1) * 4 + (w & 3); duty = w < 4;
        const int co = 6 + qi;
        WO wq;
        wq.g = wo + ct * 128;
        const unsigned hv = qkv_attn(smem, xA, wqkv, wo, ws, rt, hg, tid, w, hs, kst, ro, co,
                                     (u64)(((ks >> 1) ? Ci : Cr) + (long)((xcd >> 2) * 8 + qi) * (128 * 4096) + (ks & 1) * 2048), qi, wq);
        STAMP(0, 21);
        if (w == 0) {
            hs_rmw(hs, ka, 1ull << hg | M_OW, lane == hg);
            if (!hs_new(hs, hv, mr)) again = 1ull << hg;
        }
        if (w >= 4) {
#pragma unroll 1
            for (int t = WE; t < 14; ++t) wo_req(wq, smem + W_A + t * 4096 + (w & 3) * 1024);
            wait_vm<0>();
        }
        BARL();
        wb = W_A;
    }
    if (duty) {
        volatile LDS_AS unsigned* go = (volatile LDS_AS unsigned*)(smem + L_GO);
        if (w == 0) { if (j < 24) { hs_rmw(hs, ka, M_OW); __builtin_amdgcn_s_sleep(PD2); } hs_wait2(hs, M_OW, M_OW, 0u, ka, kb); *go = 1u; }
        else lds_wait<SL2>(L_GO);
        asm volatile("" ::: "memory");
        STAMP(1, 0);
        if (w == 0) hs_add(hs, M_OR);
        oproj(smem + wb, (u16*)(ws + WS_O), out, ro, rg, ct, lane);
        hs_keep(ka, kb);
        if (again) { hs_wait(hs, mr, mr, 0u); hs_add(hs, again); }
    }
    STAMP(1, 24);
}

extern "C" void kernel_launch(void* const* d_in, const int* in_sizes, int n_in, void* d_out, int out_size,
                              void* d_ws, size_t ws_size, hipStream_t stream) {
    kf<<<256, 512, 0, stream>>>((const u16*)d_in[0], (const u16*)d_in[1], (const char*)d_in[3], (const char*)d_in[6], (const char*)d_in[7],
                                (const char*)d_in[4], (const char*)d_in[5], (const char*)d_in[2], (u16*)d_out, (char*)d_ws);
}
```

```cpp
#include <hip/hip_runtime.h>
#include <hip/hip_bf16.h>

typedef unsigned short u16;
typedef __attribute__((ext_vector_type(8))) short bf16x8;
typedef __attribute__((ext_vector_type(4))) short s16x4;
typedef __attribute__((ext_vector_type(4))) float f32x4;
typedef __attribute__((ext_vector_type(2))) __bf16 bf2;
typedef __attribute__((ext_vector_type(2))) unsigned u32x2;
typedef __attribute__((ext_vector_type(4))) unsigned u32x4;

#define LDS_AS __attribute__((address_space(3)))
#define GLB_AS __attribute__((address_space(1)))

constexpr int D = 448;

#define STAMP(kern, idx)
#define STAMPC(kern, idx)

typedef __attribute__((ext_vector_type(2))) float f32x2;
__device__ __forceinline__ unsigned pk2(float a, float b) {
    f32x2 v = {a, b};
    return __builtin_bit_cast(unsigned, __builtin_convertvector(v, bf2));
}
__device__ __forceinline__ void glds16a(const void* sbase, unsigned voff, const char* l) {
    const unsigned la = (unsigned)(size_t)(LDS_AS const char*)l;
    asm volatile("s_mov_b32 m0, %2\n\ts_nop 0\n\tglobal_load_lds_dwordx4 %0, %1" ::"v"(voff), "s"(sbase), "s"(la) : "memory");
}
__device__ __forceinline__ float vmax(float a, float b) { return __builtin_elementwise_maximum(a, b); }
__device__ __forceinline__ float vmax3(float a, float b, float c) { return vmax(vmax(a, b), c); }
__device__ __forceinline__ bf16x8 lds_rd128(const char* p) { return *reinterpret_cast<const bf16x8*>(p); }
__device__ __forceinline__ s16x4 lds_tr(const char* p) {
    return __builtin_amdgcn_ds_read_tr16_b64_v4i16((LDS_AS s16x4*)p);
}
__device__ __forceinline__ bf16x8 cat(s16x4 a, s16x4 b) {
    bf16x8 r;
    r[0] = a[0]; r[1] = a[1]; r[2] = a[2]; r[3] = a[3];
    r[4] = b[0]; r[5] = b[1]; r[6] = b[2]; r[7] = b[3];
    return r;
}
#define MFMA(a, b, c) __builtin_amdgcn_mfma_f32_16x16x32_bf16((a), (b), (c), 0, 0, 0)
template <int N> __device__ __forceinline__ void wait_vm() { asm volatile("s_waitcnt vmcnt(%0)" ::"i"(N) : "memory"); }
#define SB() __builtin_amdgcn_sched_barrier(0)
template <int NP, int NM> __device__ __forceinline__ void ilv() {
#pragma unroll
    for (int i = 0; i < NP; ++i) {
        __builtin_amdgcn_sched_group_barrier(0x8, 1, 0);
        __builtin_amdgcn_sched_group_barrier(0x100, 1, 0);
    }
    if constexpr (NM > 0) __builtin_amdgcn_sched_group_barrier(0x8, NM, 0);
}
#define BAR()                                               \
    do {                                                    \
        asm volatile("s_waitcnt lgkmcnt(0)" ::: "memory");  \
        __builtin_amdgcn_s_barrier();                       \
        asm volatile("" ::: "memory");                      \
    } while (0)
#define BARL()                                              \
    do {                                                    \
        asm volatile("" ::: "memory");                      \
        __builtin_amdgcn_s_barrier();                       \
        asm volatile("" ::: "memory");                      \
    } while (0)

#define BARK() do { __builtin_amdgcn_s_waitcnt(0xc07f); asm volatile("" ::: "memory"); __builtin_amdgcn_s_barrier(); asm volatile("" ::: "memory"); SB(); } while (0)
__device__ __forceinline__ int kperm(int r) { return (r & ~12) | ((r & 4) << 1) | ((r & 8) >> 1); }

constexpr int LDS_TOTAL = 163840;
constexpr int WS_O = 4 << 20, WS_PX = 8 << 20, WS_HS = 16 << 20;
#define CODEMAX 41984u
constexpr unsigned CODE_MAX = CODEMAX;

typedef unsigned long long u64;
struct Hs {
    const unsigned* base;
    unsigned off, snap;
};
constexpr int HS_O = 16 * 256, HS_X = 32 * 256;
constexpr u64 M_LO = 0xffffull, M_HI = 0xffff0000ull, M_OW = 0xfull << 48, M_OR = 1ull << 52, M_R14 = 0x3fffull << 16;
constexpr u64 M_XW = 0xfull << 32, M_XR = 0xf0ull << 32;
__device__ __forceinline__ unsigned hs_off(int lane, int b, int bx, int ro, int co) {
    const int bo = HS_O + ro * 256 - 48 * 4;
    return (unsigned)(lane * 4 + (lane < 32 ? b : lane < 48 ? bx - 32 * 4 : lane == 52 ? bo + 48 + co * 4 : bo));
}
template <int N> __device__ __forceinline__ unsigned hs_take(unsigned v) {
    unsigned r;
    asm volatile("s_waitcnt vmcnt(%2)\n\tv_mov_b32 %0, %1" : "=v"(r) : "v"(v), "i"(N) : "memory");
    return r;
}
__device__ __forceinline__ void hs_rmw(const Hs& h, unsigned& v, u64 m, unsigned d = 0u) {
    asm volatile("s_mov_b64 exec, %4\n\tglobal_atomic_add %0, %1, %2, %3 sc0 sc1\n\ts_mov_b64 exec, -1" : "+v"(v) : "v"(h.off), "v"(d), "s"(h.base), "s"(m) : "memory");
}
__device__ __forceinline__ void hs_add(const Hs& h, u64 m) {
    const unsigned one = 1u;
    asm volatile("s_mov_b64 exec, %3\n\tglobal_atomic_add %0, %1, %2 sc1\n\ts_mov_b64 exec, -1" ::"v"(h.off), "v"(one), "s"(h.base), "s"(m) : "memory");
}
__device__ __forceinline__ bool hs_new(const Hs& h, unsigned v, u64 m) { return (__ballot(v == h.snap) & m) == 0; }
#define PSL 2
__device__ __forceinline__ void hs_wait(const Hs& h, u64 m, u64 e, unsigned d) {
#pragma unroll 1
    for (unsigned spin = 0; spin < 40000u; ++spin) {
        unsigned v = 0;
        hs_rmw(h, v, e, d);
        if (hs_new(h, hs_take<0>(v), m)) return;
        __builtin_amdgcn_s_sleep(PSL);
    }
}
#define PD2 5
__device__ __forceinline__ void hs_wait2(const Hs& h, u64 m, u64 e, unsigned d, unsigned& a, unsigned& b) {
    unsigned n = 40000u;
    asm volatile("s_mov_b64 exec, %8\n"
                 "1:\n\tglobal_atomic_add %1, %3, %4, %5 sc0 sc1\n\ts_waitcnt vmcnt(1)\n\tv_cmp_eq_u32 vcc, %0, %6\n\ts_and_b64 vcc, vcc, %7\n\ts_cbranch_scc0 2f\n\t"
                 "global_atomic_add %0, %3, %4, %5 sc0 sc1\n\ts_waitcnt vmcnt(1)\n\tv_cmp_eq_u32 vcc, %1, %6\n\ts_and_b64 vcc, vcc, %7\n\ts_cbranch_scc0 2f\n\t"
                 "s_sub_u32 %2, %2, 1\n\ts_cbranch_scc0 1b\n"
                 "2:\n\ts_mov_b64 exec, -1"
                 : "+&v"(a), "+&v"(b), "+&s"(n) : "v"(h.off), "v"(d), "s"(h.base), "v"(h.snap), "s"(m), "s"(e) : "memory", "vcc", "scc");
}
__device__ __forceinline__ void hs_keep(unsigned a, unsigned b) { asm volatile("s_waitcnt vmcnt(0)" ::"v"(a), "v"(b) : "memory"); }
template <int POL> __device__ __forceinline__ void st16_out(__amdgpu_buffer_rsrc_t r, unsigned off, u32x4 v) {
    __builtin_amdgcn_raw_buffer_store_b128(v, r, off, 0, POL);
}
__device__ __forceinline__ void st16_sc1(__amdgpu_buffer_rsrc_t r, unsigned off, u32x4 v) {
    __builtin_amdgcn_raw_buffer_store_b128(v, r, off, 0, 16);
}
__device__ __forceinline__ void glds16m(const void* sbase, unsigned voff, const char* l, u64 mask) {
    const unsigned la = (unsigned)(size_t)(LDS_AS const char*)l;
    asm volatile("s_mov_b32 m0, %2\n\ts_mov_b64 exec, %3\n\tglobal_load_lds_dwordx4 %0, %1\n\ts_mov_b64 exec, -1" ::"v"(voff), "s"(sbase), "s"(la), "s"(mask) : "memory");
}
__device__ __forceinline__ void glds16c(const void* sbase, unsigned voff, const char* l) {
    const unsigned la = (unsigned)(size_t)(LDS_AS const char*)l;
    asm volatile("s_mov_b32 m0, %2\n\ts_nop 0\n\tglobal_load_lds_dwordx4 %0, %1 sc1" ::"v"(voff), "s"(sbase), "s"(la) : "memory");
}
__device__ __forceinline__ void glds16mc(const void* sbase, unsigned voff, const char* l, u64 mask) {
    const unsigned la = (unsigned)(size_t)(LDS_AS const char*)l;
    asm volatile("s_mov_b32 m0, %2\n\ts_mov_b64 exec, %3\n\tglobal_load_lds_dwordx4 %0, %1 sc1\n\ts_mov_b64 exec, -1" ::"v"(voff), "s"(sbase), "s"(la), "s"(mask) : "memory");
}
template <int SL> __device__ __forceinline__ void lds_wait(unsigned a) {
    unsigned n = 1000000u, v;
    asm volatile("1:\n\ts_sleep %3\n\tds_read_b32 %0, %2\n\ts_sub_u32 %1, %1, 1\n\ts_cbranch_scc1 2f\n\ts_waitcnt lgkmcnt(0)\n\tv_cmp_eq_u32 vcc, 0, %0\n\ts_cbranch_vccnz 1b\n"
                 "2:\n\ts_waitcnt lgkmcnt(0)" : "=&v"(v), "+s"(n) : "v"(a), "i"(SL) : "memory", "vcc", "scc");
}

constexpr int A_SL = 16384, B_R0 = 6 * A_SL, B_SL = 20480, X_SL = 8192, H_SL = 10240, H_0 = 11 * X_SL, L_KEY = H_0 + 7 * H_SL, L_GO = 163328, W_0 = A_SL;
constexpr int CODE_PART = (CODE_MAX - 6144) / 7 / 128 * 128, CODE_LN = (CODE_MAX / 128 + 23) / 24;
#define TT(i)
struct WO { const char* g; u64 pat; unsigned off, hi; };
__device__ __forceinline__ void wo_init(WO& q, int lane, int w) {
    q.g += w * (8 * 896);
    q.pat = 0ull;
#pragma unroll
    for (int i = 0; i < 21; ++i) q.pat |= (u64)(i % 7 == 6 ? 7 : (i % 7 + 1) / 2) << (3 * i);
    q.pat >>= 6 * w;
    q.hi = (unsigned)(lane >> 5) * (3 * 896);
    q.off = (unsigned)(((lane >> 5) * 16 + ((lane >> 3) & 3)) * 896 + ((lane & 7) ^ ((w & 1) * 4 + ((lane >> 4) & 1) * 2)) * 16);
    asm volatile("" : "+v"(q.off), "+v"(q.hi));
}
__device__ __forceinline__ void wo_req(WO& q, const char* l) {
    glds16a(q.g - ((unsigned)q.pat & 3) * (4 * 896), q.off + ((unsigned)q.pat & 4) * q.hi, l);
    q.g += 32 * 896; q.pat >>= 3;
}
#define SL1 1
#define SL2 1
#define N4 3
#define N5 4
#define N6 5
#define KA 3
#define V1 1
#define PDG 1
#define DEF 2
#define TC0 0x76542310u
#define TC1 0x76530421u
#define TC2 0x43176502u
#define LP 2
#define WP1 3
#define WP2 10
#define WE 3
struct BL {
    const char *xg, *hg;
    unsigned offX, offH[3];
    int kc;
};
__device__ __forceinline__ void reqX(BL& q, char* smem, int w, int sx) {
    const char* gx = q.xg + q.kc * 128;
    const char* la = smem + sx * X_SL + w * 2048;
    glds16a(gx, q.offX, la);
    glds16a(gx + 8 * 896, q.offX, la + 1024);
    q.kc = q.kc == 6 ? 0 : q.kc + 1;
}
__device__ __forceinline__ void reqH(BL& q, char* smem, int w, int k) {
    const char* gh = q.hg + q.kc * 128;
    const char* lh = smem + H_0 + k * H_SL + w * 2560;
    glds16a(gh, q.offH[0], lh);
    glds16a(gh, q.offH[1], lh + 1024);
    glds16m(gh, q.offH[2], lh + 2048, 0xffffffffull);
}
__device__ __forceinline__ void b_off(BL& q, int hn, int lane, int w) {
    const int r8 = lane >> 3, c = (lane & 7) ^ r8;
    q.offX = (unsigned)((w * 16 + r8) * 896 + c * 16);
#pragma unroll
    for (int i = 0; i < 3; ++i) q.offH[i] = (unsigned)(min(w * 20 + i * 8 + r8, hn) * 896 + (c ^ ((w & 1) * 4)) * 16);
}
__device__ __forceinline__ void b_load(char* smem, BL& q, const char* cg, const char* abg, u64 tb, int ks,
                                       bool narrow, const char* wog, bool duty, Hs& hs, int lane, int w, int pm, u64 pc) {
    const int r8 = lane >> 3, c = (lane & 7) ^ r8;
    unsigned offA[4];
#pragma unroll
    for (int i = 0; i < 4; ++i)
        offA[i] = (unsigned)(((w >> 1) * 64 + (((w & 1) * 2 + (i >> 1) + ks) & 3) * 16 + (i & 1) * 8 + r8) * 4096 + c * 16);
    const int rb0 = w * (narrow ? 32 : 40);
    unsigned offB = (unsigned)((c >> 2) * 10240 + (rb0 + r8) * 64 + (c & 3) * 16);
    u64 mB = narrow ? 1ull : ~0ull;
    int sa = 3;
    u64 tc = tb | 0x8888888800000000ull;
    const unsigned t3 = (unsigned)tb;
    auto reqC = [&]() {
        const char* g = cg + ((unsigned)tc & 15) * 128;
        const char* la = smem + sa * A_SL + w * 4096;
#pragma unroll
        for (int i = 0; i < 4; ++i) glds16a(g, offA[i], la + i * 1024);
        sa = sa == 5 ? 0 : sa + 1; tc >>= 4;
    };
    int sb = 0, lb0 = B_R0 + rb0 * 128;
    tb = tb << 1 | 0x1111111100000000ull;
    auto reqB = [&]() {
        const char* g = abg + ((unsigned)tb & 15) * B_SL;
        const char* lb = smem + lb0 + sb * B_SL;
#pragma unroll
        for (int i = 0; i < 4; ++i) glds16c(g + i * 512, offB, lb + i * 1024);
        glds16mc(g + 2048, offB, lb + 4096, mB);
        sb = sb == 2 ? 0 : sb + 1; tb >>= 4;
    };
    auto step = [&]() { reqB(); reqC(); wait_vm<13>(); BARL(); };
    u64 tt[11] = {0, 0, 0, 0, 0, 0, 0, 0, 0, 0, 0};
#pragma unroll 1
    for (int k = 2; k < 7; ++k) {
        reqH(q, smem, w, k); reqX(q, smem, w, k == 6 ? 10 : k);
        wait_vm<10>();
        BARL();
    }
    q.xg += 512 * 896;
    auto reqX2 = [&](int a, int b) {
#pragma unroll
        for (int i = a; i < b; ++i) reqX(q, smem, w, i == 6 ? 10 : i);
    };
    reqX2(0, N4);
    wait_vm<5 + 2 * N4>();
    BARL();
    reqX2(N4, N5);
    wait_vm<2 * N5>();
    BARL();
    TT(0);
    reqX2(N5, N6);
    unsigned own = (lane & 15) == pm, hv = 0, hw = 0;
    u64 m3 = 3u << (t3 << 1 & 14) | 3u << (t3 >> 3 & 14) | 3u << (t3 >> 7 & 14);
    asm volatile("" : "+v"(offA[0]), "+v"(offA[1]), "+v"(offA[2]), "+v"(offA[3]), "+v"(offB), "+v"(own));
    asm volatile("" : "+s"(mB), "+s"(m3), "+s"(abg), "+s"(cg), "+s"(lb0), "+s"(tb), "+s"(tc));
    if (w == 0) {
        const u32x2 t = *reinterpret_cast<const u32x2*>(smem + L_KEY + lane * 8);
        hs.off = t[0]; hs.snap = t[1];
    }
    wait_vm<2 * (N6 - KA)>();
    BARL();
    TT(1);
    reqX2(N6, 7);
    reqC();
    if (V1) reqC();
    wait_vm<4 + 4 * V1>();
    BARL();
    TT(2);
    if (w == 0) hs_add(hs, 1ull << pm);
    if (!V1) reqC();
#define PDA 5
    if (PDA && w == 0) { __builtin_amdgcn_s_sleep(PDA); hs_rmw(hs, hv, M_LO); }
    BARL();
    TT(3);
    if (w == 0) {
        if (!PDA) hs_rmw(hs, hv, M_LO);
        __builtin_amdgcn_s_sleep(PDG);
        hs_wait2(hs, m3, M_LO, own, hv, hw);
        *(volatile LDS_AS unsigned*)(smem + L_GO + 4) = 1u;
    } else lds_wait<SL1>(L_GO + 4);
    TT(4);
    reqB(); reqB();
    unsigned h2 = 0;
    if (w == 0) hs_rmw(hs, h2, M_LO);
    reqC();
    wait_vm<9>();
    asm volatile("" ::"v"(hv), "v"(hw));
    BARL();
#define CT 0
    auto code = [&]() {
        if (w == 2) asm volatile("s_mov_b32 m0, %2\n\ts_mov_b64 exec, %3\n\tglobal_load_lds_dword %0, %1\n\ts_mov_b64 exec, -1" ::"v"((unsigned)lane * 128u), "s"(pc), "i"(162816), "s"((1ull << CODE_LN) - 1) : "memory");
    };
    if (CT & 1) code();
    reqB(); reqC();
    {
        const unsigned v = hs_take<13>(h2);
        if (w == 0) { if (!hs_new(hs, v, M_LO)) hs_wait(hs, M_LO, M_LO, own); }
    }
    BARL();
    if (w == 0) hs_add(hs, 0x10001ull << pm | (duty ? M_OR : 0ull));
#pragma unroll 1
    for (int i = 1; i < 4; ++i) step();
    if (w == 0) hs_rmw(hs, h2, M_LO | M_HI, own);
    if (CT & 2) code();
    step();
    if (w == 0) { if (!hs_new(hs, hs_take<9>(h2), M_HI)) hs_wait(hs, M_HI, M_HI, 0u); }
#pragma unroll 1
    for (int i = 5; i < 13; ++i) step();
    WO wq;
    wq.g = wog;
    wo_init(wq, lane, w);
    reqB();
    if (duty) {
#pragma unroll
        for (int n = 0; n < 14; ++n) {
            if (n == WP1) { wait_vm<9 + WP1>(); BARL(); }
            if (n == WP2) { wait_vm<WP2>(); BARL(); }
            wo_req(wq, smem + W_0 + n * 4096 + w * 1024);
        }
    } else {
        wait_vm<9>(); BARL();
        wait_vm<0>(); BARL();
    }
    wait_vm<0>();
    BARL();
}
template <int NE, class F>
__device__ __forceinline__ void ab_mma(char* smem, unsigned dbase, unsigned sg, int lane, int sh, int ef, u16* ab, F post) {
    const int fr = lane & 15, fq = lane >> 4;
    const int z0 = fr * 128 + ((fq ^ (fr & 7)) * 16), z1 = z0 ^ 64;
    unsigned a0 = sh * 4096 + z0, a1 = sh * 4096 + z1, b0 = H_0 + ef * 2048 + z0, b1 = H_0 + ef * 2048 + z1;
    unsigned doff = dbase + (unsigned)(sh * 10240 + (ef * 16 + fr) * 64 + ((fq & 1) * 2 + (fq >> 1)) * 16);
    asm volatile("" : "+v"(a0), "+v"(a1), "+v"(b0), "+v"(b1), "+v"(doff));
    const char *sa0 = smem + a0, *sa1 = smem + a1, *sb0 = smem + b0, *sb1 = smem + b1;
    const __amdgpu_buffer_rsrc_t rs = __builtin_amdgcn_make_buffer_rsrc(ab, 0, 192 * B_SL, 0x00020000);
    f32x4 acc[NE][2];
    bf16x8 xf[2][2][2], hf[2][2][NE];
    auto rd = [&](int k, int i) {
        const int h = i >= 2 + NE, n = i - h * (2 + NE);
        if (n < 2) xf[k & 1][h][n] = lds_rd128((h ? sa1 : sa0) + (k == 6 ? 10 : k) * X_SL + n * 2048);
        else hf[k & 1][h][n - 2] = lds_rd128((h ? sb1 : sb0) + k * H_SL + (n - 2) * 2048);
    };
#pragma unroll 1
    for (int p = 0; p < 2; ++p) {
#pragma unroll
        for (int a = 0; a < NE; ++a) {
            acc[a][0] = f32x4{0.f, 0.f, 0.f, 0.f}; acc[a][1] = f32x4{0.f, 0.f, 0.f, 0.f};
            asm volatile("" : "+v"(acc[a][0]), "+v"(acc[a][1]));
        }
#pragma unroll
        for (int k = 0; k < 8; ++k) {
            if (k == 0) BARL();
            else if (k == KA) { if (p) wait_vm<0>(); BARL(); }
            else if (k == 7) { if (p) BAR(); }
            else if (!p) { if (k == 6) { post(); BAR(); } else BARL(); }
            if (k == 0 && p == 0) STAMP(0, 1);
            SB();
#pragma unroll
            for (int i = 0; i < 4 * NE; ++i) {
                const int h = i >= 2 * NE, n = i - h * 2 * NE;
                if (k == 7) acc[n >> 1][n & 1] = MFMA(xf[0][h][n & 1], hf[0][h][n >> 1], acc[n >> 1][n & 1]);
                else if (k) asm volatile("v_mfma_f32_16x16x32_bf16 %0, %1, %2, %0" : "+v"(acc[n >> 1][n & 1]) : "v"(xf[~k & 1][h][n & 1]), "v"(hf[~k & 1][h][n >> 1]));
                SB();
                if (k < 7 && i < 4 + 2 * NE) rd(k, i);
                SB();
            }
        }
#pragma unroll
        for (int mi = 0; mi < NE; ++mi) {
            const f32x4 a = acc[mi][0], b = acc[mi][1];
            auto x = __builtin_amdgcn_permlane16_swap(pk2(a[0], a[1]) ^ sg, pk2(b[0], b[1]) ^ sg, false, false);
            auto y = __builtin_amdgcn_permlane16_swap(pk2(a[2], a[3]) ^ sg, pk2(b[2], b[3]) ^ sg, false, false);
            st16_sc1(rs, doff + mi * 1024u, u32x4{x[0], y[0], x[1], y[1]});
        }
        SB();
        doff += B_SL;
    }
}
template <int NF>
__device__ __forceinline__ void y_mma(char* smem, int lane, int cw, u16* px, unsigned pxb, int ks, u16* out, unsigned ob0, Hs& hs) {
    const int fr = lane & 15, fq = lane >> 4;
    const int mh = cw >> 1, nh = cw & 1;
    f32x4 acc[5][4];
#pragma unroll
    for (int a = 0; a < NF; ++a)
#pragma unroll
        for (int b = 0; b < 4; ++b) { acc[a][b] = f32x4{0.f, 0.f, 0.f, 0.f}; asm volatile("" : "+v"(acc[a][b])); }
    const int z0 = fr * 128 + ((fq ^ (fr & 7)) * 16), z1 = z0 ^ 64;
    unsigned a0 = 3 * A_SL + mh * 8192 + z0, a1 = 3 * A_SL + mh * 8192 + z1, b0 = B_R0 + nh * (NF * 2048) + z0, b1 = B_R0 + nh * (NF * 2048) + z1;
    unsigned pl = pxb + (unsigned)(cw * 8192 + lane * 16), ph = pl + (unsigned)(6144 - 512 - lane * 8);
    if (NF == 5) asm volatile("" : "+v"(ph));
    unsigned ob = ob0 + (unsigned)((mh * 64 + fr) * 896 + nh * (NF * 16) + (fq & 1) * 16 + (fq >> 1) * 8) * 2u;
    const unsigned one = fq == (fr >> 2) ? 0x3f80u << ((fr & 1) * 16) : 0u, d0 = (fr & 2) ? 0u : one, d1 = (fr & 2) ? one : 0u;
    u32x4 s0 = {d0, d1, 0u, 0u}, s1 = {0u, 0u, d0, d1};
    unsigned own = lane == 32 + ks;
    asm volatile("" : "+v"(a0), "+v"(a1), "+v"(b0), "+v"(b1), "+v"(pl), "+v"(ob), "+v"(s0), "+v"(s1), "+v"(own));
    const char *sa0 = smem + a0, *sa1 = smem + a1, *sb0 = smem + b0, *sb1 = smem + b1;
    const __amdgpu_buffer_rsrc_t rs = __builtin_amdgcn_make_buffer_rsrc(px, 0, 48 * 4 * 32768, 0x00020000);
    const __amdgpu_buffer_rsrc_t rs_out = __builtin_amdgcn_make_buffer_rsrc(out, 0, 2048 * 896 * 2, 0x00020000);
    bf16x8 cf[2][4], xf[2][NF];
    auto rdx = [&](int mi, int k, int st) { xf[k][mi] = lds_rd128((k ? sb1 : sb0) + st * B_SL + mi * 2048); };
    auto rdc = [&](int l, int k, int st) { cf[k][l] = lds_rd128((k ? sa1 : sa0) + st * A_SL + l * 2048); };
    auto mf = [&](int mi, int l, int k) {
        asm volatile("v_mfma_f32_16x16x32_bf16 %0, %1, %2, %0" : "+v"(acc[mi][l]) : "v"(xf[k][mi]), "v"(cf[k][l]));
        SB();
    };
    int da = -3 * A_SL;
    auto head = [&](int st, int lo) {
        int n = 0;
        __builtin_amdgcn_s_waitcnt(0xc47f);
        SB();
#pragma unroll
        for (int i = 4 * lo; i < 16; ++i) {
            mf((i >> 1) & 1, i >> 2, i & 1);
            if (n < 2 * NF - 4) rdx(2 + (n >> 1), n & 1, st);
            if (st == 2 && n == 8) sa0 += da;
            if (st == 2 && n == 10) { sa1 += da; da = -da; }
            ++n;
            SB();
        }
        if (NF == 5) {
#pragma unroll
            for (int i = 2 * lo; i < 8; ++i) mf(2, i >> 1, i & 1);
        }
    };
    auto rest = [&](int st, int lo) {
#pragma unroll
        for (int i = 4 * lo; i < 16; ++i) {
            mf(NF - 2 + ((i >> 1) & 1), i >> 2, i & 1);
            if ((i & 3) >= 2) rdc(i >> 2, i & 1, st);
            else if (i - 4 * lo < 8) rdx((i >> 2) - lo, i & 1, st);
            else if (lo) rdc(0, i & 1, st);
            SB();
        }
    };
    STAMP(0, 4);
    BARL();
    STAMP(0, 5); STAMPC(0, 3);
#pragma unroll
    for (int i = 0; i < 16; ++i) {
        if ((i & 3) >= 2) rdc(i >> 2, i & 1, 0);
        else if (i < 8) rdx(i >> 2, i & 1, 0);
        SB();
    }
#pragma unroll 1
    for (int t = 0; t < 4; ++t) {
        head(0, 0);
        wait_vm<0>();
        BARK();
        rest(1, 0); head(1, 0);
        BARK();
        rest(2, 0); head(2, 0);
        BARK();
        rest(0, 0);
    }
    bf16x8 kf[4];
    unsigned ka = 0, kb = 0;
#define D3 0
    bf16x8 kx[2][NF], kc[2];
    head(0, D3); BARK();
    if (D3) {
#pragma unroll
        for (int i = 0; i < 2 * NF; ++i) kx[i & 1][i >> 1] = xf[i & 1][i >> 1];
        kc[0] = cf[0][0]; kc[1] = cf[1][0];
    }
    rest(1, D3); head(1, 1); BARK();
    kf[0] = cf[0][0]; kf[1] = cf[1][0];
    rest(2, 1); head(2, 1); BARK();
    hs_rmw(hs, kb, M_XR);
    kf[2] = cf[0][0]; kf[3] = cf[1][0];
    rest(0, 1);
    STAMP(0, 18); STAMPC(0, 4);
    const int pw = ks * 32768;
    auto put = [&](int p, int h) {
        const int sl = 2 * (p - 1) + h;
        const f32x4 a = acc[h < 2 ? 2 * h : NF - 1][p], b = acc[h < 2 ? 2 * h + 1 : 0][p];
        if (h < 2) __builtin_amdgcn_raw_buffer_store_b128(u32x4{pk2(a[0], a[1]), pk2(a[2], a[3]), pk2(b[0], b[1]), pk2(b[2], b[3])}, rs, pl + (sl & 3) * 1024u, pw + (sl >> 2 << 12), 16);
        else __builtin_amdgcn_raw_buffer_store_b64(u32x2{pk2(a[0], a[1]), pk2(a[2], a[3])}, rs, ph + p * 512u, pw, 16);
    };
    constexpr int NG = 2 * NF - 4, NL = 12 + 3 * NG;
#pragma unroll
    for (int i = 0; i < NL + 2 * NF; ++i) {
        const int j = i - 12, l = i < 12 ? 1 + (i >> 2) : i < NL ? 1 + j / NG : 0, mi = i < 12 ? (i >> 1) & 1 : i < NL ? 2 + j % NG / 2 : (i - NL) >> 1;
        acc[mi][l] = MFMA(xf[i & 1][mi], cf[i & 1][l], acc[mi][l]);
        SB();
        if (i < NG) rdx(2 + (i >> 1), i & 1, 0);
        const int e = i - 2;
        if (e == 4 || e == 8 || e == 12) put(e >> 2, 0);
        if (e > 12 && e <= NL && (e - 12) % NG == 4 % NG) put((e - 13) / NG + 1, 1);
        if (NF == 5 && e > 12 && e <= NL && (e - 12) % NG == 0) put((e - 12) / NG, 2);
        if (i > NL + 1) rdx((i - NL - 2) >> 1, i & 1, 1);
        if (i == NL + 3) BARL();
        SB();
    }
    rdx(NF - 1, 0, 1); rdx(NF - 1, 1, 1);
#pragma unroll
    for (int i = 0; i < 4 * NF; ++i) {
        const int mi = (i >> 1) % NF;
        acc[mi][0] = MFMA(xf[i & 1][mi], kf[(i >= 2 * NF) * 2 + (i & 1)], acc[mi][0]);
        SB();
        if (i < 2 * NF) rdx(mi, i & 1, 2);
        SB();
    }
#pragma unroll
    for (int mi = 0; mi < NF; ++mi) asm volatile("" : "+v"(acc[mi][0]));
    STAMP(0, 19);
    const bool all = hs_new(hs, hs_take<0>(kb), M_XR);
    STAMP(0, 20);
    hs_rmw(hs, ka, M_XW, own);
    if (D3) {
#pragma unroll
        for (int i = 0; i < 2 * NF; ++i) { acc[i >> 1][0] = MFMA(kx[i & 1][i >> 1], kc[i & 1], acc[i >> 1][0]); SB(); }
#pragma unroll
        for (int mi = 0; mi < NF; ++mi) asm volatile("" : "+v"(acc[mi][0]));
    }
    int pr[4];
#pragma unroll
    for (int l = 1; l < 4; ++l) pr[l - 1] = ((ks - l) & 3) * 32768 + (l == 3 ? 4096 : 0);
    pr[3] = pr[2] - 4096;
    unsigned pl2 = pl, ph2 = ph;
    u32x4 s01 = s0 | s1;
    __amdgpu_buffer_rsrc_t ro2 = rs_out;
    asm volatile("" : "+s"(pr[0]), "+s"(pr[1]), "+s"(pr[2]), "+s"(pr[3]), "+s"(ro2));
    asm volatile("" : "+v"(pl2), "+v"(ph2), "+v"(s01));
    auto fin = [&](int pr) {
        const f32x4 a = acc[2 * pr][0], b = acc[pr < 2 ? 2 * pr + 1 : 2 * pr][0];
        auto x = __builtin_amdgcn_permlane16_swap(pk2(a[0], a[1]), pk2(b[0], b[1]), false, false);
        auto y = __builtin_amdgcn_permlane16_swap(pk2(a[2], a[3]), pk2(b[2], b[3]), false, false);
        if (pr < 2 || !(fq & 1)) st16_out<0>(ro2, ob + pr * 64u, u32x4{x[0], y[0], x[1], y[1]});
        SB();
    };
    __builtin_amdgcn_s_sleep(PD2 - 2 * D3);
    hs_wait2(hs, M_XW ^ 1ull << (32 + ks), M_XW ^ 1ull << (32 + ks), 0u, ka, kb);
    STAMP(1, 0);
    bf16x8 pv[2][3];
    u32x2 p5[3];
#pragma unroll
    for (int h = 0; h < (NF + 1) / 2; ++h)
#pragma unroll
        for (int l = 1; l < 4; ++l) {
            const int sl = 2 * (l - 1) + h;
            if (h < 2) pv[h][l - 1] = __builtin_bit_cast(bf16x8, __builtin_amdgcn_raw_buffer_load_b128(rs, pl2 + (sl & 3) * 1024u, pr[l - 1], 16));
            else p5[l - 1] = __builtin_amdgcn_raw_buffer_load_b64(rs, ph2 + l * 512u, pr[l == 3 ? 3 : l - 1], 16);
            SB();
        }
    if (!all) hs_add(hs, 1ull << (32 + ks));
#pragma unroll
    for (int h = 0; h < (NF + 1) / 2; ++h) {
        if (h < 2) {
#pragma unroll
            for (int l = 0; l < 3; ++l) {
                acc[2 * h][0] = MFMA(__builtin_bit_cast(bf16x8, s0), pv[h][l], acc[2 * h][0]);
                acc[2 * h + 1][0] = MFMA(__builtin_bit_cast(bf16x8, s1), pv[h][l], acc[2 * h + 1][0]);
                SB();
                if (h == 1 && l == 1 && NF == 5) fin(0);
            }
        } else {
            acc[4][0] = MFMA(__builtin_bit_cast(bf16x8, s01), __builtin_bit_cast(bf16x8, u32x4{p5[0][0], p5[0][1], p5[1][0], p5[1][1]}), acc[4][0]);
            acc[4][0] = MFMA(__builtin_bit_cast(bf16x8, s0), __builtin_bit_cast(bf16x8, u32x4{p5[2][0], p5[2][1], 0u, 0u}), acc[4][0]);
            SB();
        }
    }
    STAMP(1, 1);
    if (NF == 4) {
        u32x4 v[2], p0, p1;
#pragma unroll
        for (int pr = 0; pr < 2; ++pr) {
            const f32x4 a = acc[2 * pr][0], b = acc[2 * pr + 1][0];
            auto x = __builtin_amdgcn_permlane16_swap(pk2(a[0], a[1]), pk2(b[0], b[1]), false, false);
            auto y = __builtin_amdgcn_permlane16_swap(pk2(a[2], a[3]), pk2(b[2], b[3]), false, false);
            v[pr] = u32x4{x[0], y[0], x[1], y[1]};
        }
#pragma unroll
        for (int i = 0; i < 4; ++i) {
            p0[i] = __builtin_amdgcn_update_dpp(v[0][i], v[1][i], 0x128, 0xf, 0xc, false);
            p1[i] = __builtin_amdgcn_update_dpp(v[1][i], v[0][i], 0x128, 0xf, 0x3, false);
        }
        const unsigned o2 = ob - (unsigned)(fr & 8) * (1792 - 8);
        st16_out<16>(ro2, o2, p0);
        st16_out<16>(ro2, o2 + 8 * 1792u, p1);
    } else
    fin(1);
    if (NF == 5) fin(2);
    hs_keep(ka, kb);
}

constexpr int Q_ASL = 18432, Q_WR = 3 * Q_ASL, Q_WSL = 21504, GP = 1216, Q_DUMMY = 162816, W_A = Q_DUMMY - 14 * 4096, Q_TAB = 84 * GP;
__device__ __forceinline__ void qkv_reqW(char* smem, const char* wg, unsigned off, int c, int kw, int sw) {
    const char* g = wg + (long)kw * (32 * 2688);
    const char* sb = smem + Q_WR + sw * Q_WSL + c * 1792;
#pragma unroll
    for (int k = 0; k < 6; ++k) glds16m(g + (k & 1) * (8 * 2688) + (k >> 1) * 896, off, sb + (k & 1) * 896 + (k >> 1) * 7168, 0xffffffffffffffull);
}
struct QX { unsigned off; int r0, r1, r2; };
__device__ __forceinline__ QX qkv_offX(int c, int lane, int s0) {
    QX q;
    q.off = (unsigned)((lane >> 3) * 1792 + ((lane & 7) ^ (lane >> 3)) * 16);
    q.r0 = max(s0 + (c < 2 ? c - 16 : 16 * c - 32), 0) * 896;
    q.r1 = (s0 + 16 * c + 32) * 896;
    q.r2 = q.r1 + (c < 2 ? 64 : -63) * 896;
    return q;
}
__device__ __forceinline__ void qkv_reqX(char* smem, const u16* __restrict__ xA, const QX& q, int c, int kx, int sx) {
    const char* g = (const char*)xA + kx * 128;
    const char* sb = smem + sx * Q_ASL + c * 1024;
    glds16a(g + q.r0, q.off, sb);
    glds16a(g + q.r1, q.off, sb + 4096);
    glds16a(g + q.r2, q.off, sb + 2 * 4096);
    glds16a(g + q.r1 + 896, q.off, sb + 3 * 4096);
    if (c < 2) glds16a(g + q.r1 + 65 * 896, q.off, sb + 4 * 4096);
}
template <int V> struct QS {
    static constexpr int col(int i) { constexpr int t[3][6] = {{0, 1, 2, 5, 3, 4}, {2, 3, 1, 0, 9, 10}, {0, 1, 2, 2, 3, 4}}; return t[V][i]; }
    static constexpr int wof(int i) { return V == 1 ? ((col(i) + 5) / 7) * 7168 + ((col(i) + 5) % 7 - 5) * 32 : col(i) * 32; }
    static constexpr int lo(int i) { return V == 0 ? (i == 3 ? 3 : 1) : V == 1 ? (i == 2 || i == 3 ? 1 : 0) : 0; }
    static constexpr int hi(int i) { return i != 3 || V == 0 ? 9 : V == 1 ? 3 : 0; }
};
#define RS2 0
#define LB 1
#define LC 3
#define LW 5
template <int V>
__device__ __forceinline__ void qkv_mma(char* smem, int lane, int G0, u64 (&tt)[11]) {
    typedef QS<V> S;
    const int fr = lane & 15, fq = lane >> 4;
    const int u = fq & 1, ah = fq >> 1, q = (lane >> 2) & 3, p = lane & 3;
    constexpr int R0 = V == 0 ? 1 : 0;
    f32x4 acc[6][9];
    unsigned aw = Q_WR + Q_WSL + (16 * ah + 4 * u + q) * 224 + 8 * p + (V < 2 ? G0 * 32 : (G0 >> 3) * 7168 + 64);
    unsigned ax0 = fr * 128 + ((fq ^ (fr & 7)) * 16), ax1 = ax0 ^ 64;
    unsigned ai = (G0 * 4 + fq) * GP + fr * 8, ah0 = ai + (fr & 8) * 64;
#pragma unroll
    for (int i = 0; i < 6; ++i)
#pragma unroll
        for (int r = S::lo(i); r < S::hi(i); ++r) { acc[i][r] = f32x4{0.f, 0.f, 0.f, 0.f}; asm volatile("" : "+v"(acc[i][r])); }
    asm volatile("" : "+v"(aw), "+v"(ax0), "+v"(ax1), "+v"(ai));
    if (V) asm volatile("" : "+v"(ah0));
    const char *pw = smem + aw, *px0 = smem + ax0, *px1 = smem + ax1;
    bf16x8 xf[9];
    s16x4 wl[6], wh[6];
    auto rdw = [&](int i, int h) {
        if (S::hi(i) == 0) return;
        if (h) wh[i] = lds_tr(pw + S::wof(i) + 8 * 224); else wl[i] = lds_tr(pw + S::wof(i));
    };
    int cw = 0x2108, cx = 0x24, dw = 0, dx = 0;
    auto tick = [&](int kind, bool extra) {
#pragma unroll
        for (int g = 0; g < 3; ++g) {
            if (g == 2 && (kind == 1 || extra)) BARK();
            int n = 0;
#pragma unroll
            for (int r = 0; r < 9; ++r)
#pragma unroll
                for (int e = 0; e < 2; ++e) {
                    const int i = 2 * g + e;
                    if (r < S::lo(i) || r >= S::hi(i)) continue;
                    asm volatile("v_mfma_f32_16x16x32_bf16 %0, %1, %2, %0" : "+v"(acc[i][r]) : "v"(cat(wl[i], wh[i])), "v"(xf[r]));
                    SB();
                    if (g < 2 ? n < 4 : (n < 8 && !(n & 1))) { const int m = g < 2 ? n : n >> 1; rdw(2 * ((g + 1) % 3) + (m >> 1), m & 1); }
                    if (g == 2 && e == 1) xf[r] = lds_rd128((kind ? px0 : px1) + r * 2048);
                    if (g == 0) {
                        if (n == 4) { dw = cw & 1 ? -4 * Q_WSL : Q_WSL; asm volatile("" : "+s"(dw)); }
                        if (n == 6) { cw >>= 1; asm volatile("" : "+s"(cw)); }
                        if (n == 8 && kind) { dx = cx & 1 ? -2 * Q_ASL : Q_ASL; asm volatile("" : "+s"(dx)); }
                        if (n == 10 && kind) { cx >>= 1; asm volatile("" : "+s"(cx)); px0 += dx; }
                        if (n == 12 && kind) px1 += dx;
                    }
                    if (g == 1 && n == 5) pw += dw;
                    ++n;
                    SB();
                }
        }
    };
    auto last = [&]() {
        int fi[48], fw[48], nf = 0, na = 99;
        u32x2 cv[48];
        auto behind = [&](int f) {
            const int a = f - LW, b = f - LC;
            if (a >= 0 && a < nf) {
                const int r = fw[a];
                *reinterpret_cast<u32x2*>(smem + (r ? ai : ah0) + S::col(fi[a]) * 4 * GP + (r == 0 ? 0 : r < 5 ? 128 * r - 64 : 128 * r)) = cv[a];
                SB();
            }
            if (b >= 0 && b < nf) { const f32x4 d = acc[fi[b]][fw[b]]; cv[b] = u32x2{pk2(d[0], d[1]), pk2(d[2], d[3])}; asm volatile("" : "+v"(cv[b])); }
            if (a == na + LB) { SB(); BARK(); }
        };
#pragma unroll
        for (int h = 0; h < (RS2 ? 6 : 3); ++h) {
            const int g = h % 3;
            int n = 0;
            if (h == 3) na = nf - 1;
#pragma unroll
            for (int r = 0; r < 9; ++r)
#pragma unroll
                for (int e = 0; e < 2; ++e) {
                    const int i = 2 * g + e;
                    if (r < S::lo(i) || r >= S::hi(i) || (RS2 && (0x67 >> r & 1) != (h < 3))) continue;
                    asm volatile("v_mfma_f32_16x16x32_bf16 %0, %1, %2, %0" : "+v"(acc[i][r]) : "v"(cat(wl[i], wh[i])), "v"(xf[r]));
                    SB();
                    if (n < 2 && h < 2) { rdw(2 * h + 2 + n, 0); rdw(2 * h + 2 + n, 1); }
                    fi[nf] = i; fw[nf] = r; ++nf;
                    behind(nf - 1);
                    ++n;
                    SB();
                }
        }
        asm volatile("s_nop 7");
#pragma unroll
        for (int f = 0; f < LW; ++f) { behind(nf + f); SB(); }
    };
    TT(9);
    __builtin_amdgcn_s_setprio(LP);
    BARL();
    TT(0); TT(5);
#pragma unroll
    for (int r = R0; r < 9; ++r) {
        if (r - R0 < 4) { rdw((r - R0) >> 1, (r - R0) & 1); SB(); }
        xf[r] = lds_rd128(px0 + r * 2048);
        SB();
    }
#pragma unroll 1
    for (int s = 0; s < 6; ++s) { tick(0, s == 0); tick(1, false); }
    TT(1); TT(6);
    tick(0, false);
    BARK();
    TT(2);
    last();
    TT(3); TT(7);
}

struct AMk { f32x4 c0, c1; u32x4 m; };
__device__ __forceinline__ AMk attn_mask(unsigned ln, bool top) {
    AMk t;
    const int out = (int)(~((0x1feu << (ln & 15)) & (top ? ~0xffu : ~0u)) >> (4 * (ln >> 4)));
    unsigned b0[4], b1[4];
#pragma unroll
    for (int i = 0; i < 4; ++i) {
        b0[i] = (unsigned)__builtin_amdgcn_sbfe(out, i, 1); b1[i] = (unsigned)__builtin_amdgcn_sbfe(out, 16 + i, 1);
        t.c0[i] = __uint_as_float(b0[i] & 0xf149f2cau);
        t.c1[i] = __uint_as_float(b1[i] & 0xf149f2cau);
    }
#pragma unroll
    for (int i = 0; i < 2; ++i) {
        t.m[i] = ~((b0[2 * i] & 0xffffu) | (b0[2 * i + 1] & 0xffff0000u));
        t.m[2 + i] = ~((b1[2 * i] & 0xffffu) | (b1[2 * i + 1] & 0xffff0000u));
    }
    asm volatile("" : "+v"(t.c0), "+v"(t.c1), "+v"(t.m));
    return t;
}
struct AA { u32x4 a; u32x2 b; };
__device__ __forceinline__ AA attn_addr(unsigned ln, int w, int rt, int hg) {
    AA t;
    const unsigned fr = ln & 15, fq = ln >> 4, b = (w >> 1) * 16, z = (w & 1) * 576, gq = ((fq & 1) * 2 + (fq >> 1)) * GP + z, gv = (56 + (ln & 3)) * GP + z;
    t.a[0] = gq + (b + fr) * 8;
    t.a[1] = fq == 3 ? 1152 - 64 : t.a[0];
    t.a[2] = gq + (b + 16 + min(fr, 7u)) * 8;
    t.a[3] = gv + (b + (ln >> 2)) * 8;
    t.b[0] = gv + (b + 16 + min(ln >> 2, 7u)) * 8;
    t.b[1] = (unsigned)(rt * 56 + hg * 14 + fq) * 2048u + (w * 16 + fr) * 16u;
    asm volatile("" : "+v"(t.a), "+v"(t.b));
    return t;
}
#define Q5 tp(0)
#define Q5N 1
#define Q6 tp(1)
#define Q7 tp(2); tp(3)
#define Q8 tw()
#define QD 0
__device__ __forceinline__ unsigned qkv_attn(char* smem, const u16* xA, const u16* wqkv, const char* wo, char* ws, int rt, int hg, int tid, int w,
                                         Hs& hs, int kst, int ro, int co, u64 cb, int qi, WO& wq) {
    const int lane = tid & 63, s0 = rt * 128, c = w & 3;
    auto st = [&](int i) { const int k = kst + i; return k >= 7 ? k - 7 : k; };
    u16* o = (u16*)(ws + WS_O);
    AMk am;
    AA aa;
    unsigned hv2 = 0;
    u64 tt[11] = {0, 0, 0, 0, 0, 0, 0, 0, 0, 0, 0};
    if (w >= 4) {
        if (w == 4) qkv_mma<0>(smem, lane, 0, tt);
        else if (w == 5) qkv_mma<1>(smem, lane, 5, tt);
        else qkv_mma<2>(smem, lane, 7 * w - 33, tt);
        __builtin_amdgcn_s_setprio(0);
        const char* tb = smem + W_A + c * 1024 + lane * 16;
        aa.a = *reinterpret_cast<const u32x4*>(tb); aa.b = *reinterpret_cast<const u32x2*>(tb + 4096);
        tb = smem + Q_TAB + lane * 16;
        am.c0 = *reinterpret_cast<const f32x4*>(tb); am.c1 = *reinterpret_cast<const f32x4*>(tb + 1024);
        am.m = *reinterpret_cast<const u32x4*>(tb + 2048);
        BAR();
        TT(4);
    } else {
#define LP0 1
        __builtin_amdgcn_s_setprio(LP0);
        const char* wg = (const char*)wqkv + ((c >> 1) * 16 + (c & 1) * 4) * 2688 + hg * 224;
        const unsigned offW = (unsigned)(lane * 16 + (lane * 37 >> 9) * (2688 - 224));
        if (QD) __builtin_amdgcn_s_sleep(QD);
        TT(0);
#define XF 0
        if (!XF) { qkv_reqW(smem, wg, offW, c, 2 * kst, 1); SB(); }
        const QX qx = qkv_offX(c, lane, s0);
        qkv_reqX(smem, xA, qx, c, kst, 0);
        if (XF) { SB(); qkv_reqW(smem, wg, offW, c, 2 * kst, 1); }
        TT(1);
        qkv_reqW(smem, wg, offW, c, 2 * kst + 1, 2);
        TT(2);
        wait_vm<6>();
        TT(4);
        BARL();
        __builtin_amdgcn_s_setprio(0);
        STAMP(0, 1);
        unsigned hv = 0;
        if (w == 0) { hs.off = hs_off(lane, HS_O + rt * 256, 0, ro, co); hs_rmw(hs, hv, M_R14 | M_OW); }
#define XO 0
        const bool late = XO == 1 || (XO == 2 && ((hg + 1) & 2));
        if (!late) qkv_reqX(smem, xA, qx, c, st(1), 1);
        qkv_reqW(smem, wg, offW, c, 2 * st(1), 3);
        if (late) wait_vm<6>(); else wait_vm<10>();
        BARL();
        STAMP(0, 9);
        int sa = 4, sx = late ? 1 : 2;
        const unsigned tc = (unsigned)((c * 32 + (lane >> 1)) * 4096), ts = (lane & 1) * 4;
        const unsigned tb = (0x76543210u + qi * 0x11111111u) & 0x77777777u | 0x88888888u;
        auto tp = [&](int i) { asm volatile("s_mov_b32 m0, %2\n\ts_nop 0\n\tglobal_load_lds_dword %0, %1" ::"v"(tc + ((tb >> 8 * i >> ts & 15) << 7)), "s"(cb), "i"(Q_DUMMY) : "memory"); };
#pragma unroll 1
        for (int s = 0; s < 5; ++s) {
            if (late) qkv_reqX(smem, xA, qx, c, st(s + 1), sx);
            qkv_reqW(smem, wg, offW, c, 2 * st(s + 1) + 1, sa);
            qkv_reqW(smem, wg, offW, c, 2 * st(s + 2), sa == 4 ? 0 : sa + 1);
            if (late) wait_vm<6>();
            else {
                qkv_reqX(smem, xA, qx, c, st(s + 2), sx);
                if (c < 2) wait_vm<11>(); else wait_vm<10>();
            }
            sa = sa >= 3 ? sa - 3 : sa + 2; sx = sx == 2 ? 0 : sx + 1;
            if (w == 0 && s == 3) { hs.snap = hs_take<63>(hv); hs_add(hs, M_OR); }
            BARL();
        }
        if (late) qkv_reqX(smem, xA, qx, c, st(6), 0);
        qkv_reqW(smem, wg, offW, c, 2 * st(6) + 1, 4);
        int l2 = lane;
        Q5;
        asm volatile("" : "+v"(l2));
        __builtin_assume(l2 >= 0 && l2 < 64);
        am = attn_mask(l2, rt + (w >> 1) == 0);
        aa = attn_addr(l2, w, rt, hg);
        const AA aa2 = attn_addr(l2, w + 4, rt, hg);
        unsigned wt = ((qi * 8 + w) * 49 + min(l2, 48)) * 128;
        asm volatile("" : "+v"(wt));
        auto tw = [&]() {
            asm volatile("s_mov_b32 m0, %2\n\ts_nop 0\n\tglobal_load_lds_dword %0, %1" ::"v"(wt), "s"(wo), "i"(Q_DUMMY) : "memory");
            asm volatile("s_mov_b32 m0, %2\n\ts_nop 0\n\tglobal_load_lds_dword %0, %1" ::"v"(wt), "s"(wo + 4 * 49 * 128), "i"(Q_DUMMY) : "memory");
        };
        wait_vm<Q5N>();
        BARL();
        Q6;
        {
            char* tq = smem + W_A + c * 1024 + l2 * 16;
            *reinterpret_cast<u32x4*>(tq) = aa2.a; *reinterpret_cast<u32x2*>(tq + 4096) = aa2.b;
            if (w == 3) {
                tq = smem + Q_TAB + l2 * 16;
                *reinterpret_cast<f32x4*>(tq) = am.c0; *reinterpret_cast<f32x4*>(tq + 1024) = am.c1; *reinterpret_cast<u32x4*>(tq + 2048) = am.m;
            }
        }
        BAR();
        if (w == 3) *reinterpret_cast<u32x2*>(smem + ((l2 & 3) * 7 + 4) * GP + 1152) = u32x2{0u, 0u};
        Q7;
        if (w == 0) hs_rmw(hs, hv2, M_R14);
        TT(3);
        BAR();
        Q8;
    }
    const unsigned m0[2] = {am.m[0], am.m[1]}, m1[2] = {am.m[2], am.m[3]};
    const f32x4 c0 = am.c0, c1 = am.c1;
    const float scale = 0.18898223650461361f, log2e = 1.4426950408889634f;
#define FTA(i)
    FTA(0);
    auto rd64 = [&](unsigned a, int g) { return *reinterpret_cast<const s16x4*>(smem + a + g * GP); };
    bf16x8 qf[4], k0[4], k1[4], v0[4], v1[4];
#pragma unroll
    for (int hh = 0; hh < 4; ++hh) {
        qf[hh] = cat(rd64(aa.a[0] + 64, 7 * hh), rd64(aa.a[1] + 64, 7 * hh + 4));
        k0[hh] = cat(rd64(aa.a[0], 28 + 7 * hh), rd64(aa.a[0], 32 + 7 * hh));
        k1[hh] = cat(rd64(aa.a[2], 28 + 7 * hh), rd64(aa.a[2], 32 + 7 * hh));
        SB();
    }
#pragma unroll
    for (int hh = 0; hh < 4; ++hh) {
        v0[hh] = cat(lds_tr(smem + aa.a[3] + 7 * hh * GP), lds_tr(smem + aa.b[0] + 7 * hh * GP));
        v1[hh] = cat(lds_tr(smem + aa.a[3] + (7 * hh + 4) * GP), lds_tr(smem + aa.b[0] + (7 * hh + 4) * GP));
    }
    SB();
    FTA(1);
#define AB2 1
    if (RS2 && AB2 == 0 && w < 4) { SB(); BARL(); SB(); }
    float l[2][4], mx[2];
#pragma unroll
    for (int hp = 0; hp < 2; ++hp) {
        const f32x4 a0 = MFMA(k0[2 * hp], qf[2 * hp], c0), a1 = MFMA(k1[2 * hp], qf[2 * hp], c1);
        const f32x4 b0 = MFMA(k0[2 * hp + 1], qf[2 * hp + 1], c0), b1 = MFMA(k1[2 * hp + 1], qf[2 * hp + 1], c1);
        float t[4];
#pragma unroll
        for (int i = 0; i < 4; ++i) {
            auto r = __builtin_amdgcn_permlane32_swap(__float_as_uint(vmax(a0[i], a1[i])), __float_as_uint(vmax(b0[i], b1[i])), false, false);
            t[i] = vmax(__uint_as_float(r[0]), __uint_as_float(r[1]));
        }
#pragma unroll
        for (int i = 0; i < 2; ++i) {
            unsigned a = pk2(t[2 * i], t[2 * i + 1]);
            a = pk2(__uint_as_float(a << 16) * scale, __uint_as_float(a & 0xffff0000u) * scale);
            l[hp][2 * i] = __uint_as_float(a << 16);
            l[hp][2 * i + 1] = __uint_as_float(a & 0xffff0000u);
        }
        mx[hp] = vmax(vmax3(l[hp][0], l[hp][1], l[hp][2]), l[hp][3]);
    }
    {
        auto r = __builtin_amdgcn_permlane16_swap(__float_as_uint(mx[0]), __float_as_uint(mx[1]), false, false);
        const float c = vmax(__uint_as_float(r[0]), __uint_as_float(r[1])) * -log2e;
        auto q2 = __builtin_amdgcn_permlane16_swap(__float_as_uint(c), __float_as_uint(c), false, false);
        mx[0] = __uint_as_float(q2[0]); mx[1] = __uint_as_float(q2[1]);
    }
    if (RS2 && AB2 == 1 && w < 4) { SB(); BARL(); SB(); }
    float sm[2];
#pragma unroll
    for (int hp = 0; hp < 2; ++hp) {
#pragma unroll
        for (int i = 0; i < 4; ++i) l[hp][i] = __builtin_amdgcn_exp2f(__builtin_fmaf(l[hp][i], log2e, mx[hp]));
        sm[hp] = (l[hp][0] + l[hp][1]) + (l[hp][2] + l[hp][3]);
    }
    {
        auto r = __builtin_amdgcn_permlane16_swap(__float_as_uint(sm[0]), __float_as_uint(sm[1]), false, false);
        const float c = __builtin_amdgcn_rcpf(__uint_as_float(r[0]) + __uint_as_float(r[1]));
        auto q2 = __builtin_amdgcn_permlane16_swap(__float_as_uint(c), __float_as_uint(c), false, false);
        sm[0] = __uint_as_float(q2[0]); sm[1] = __uint_as_float(q2[1]);
    }
    FTA(2);
    const f32x4 z = {0.f, 0.f, 0.f, 0.f};
    const __amdgpu_buffer_rsrc_t rs_o = __builtin_amdgcn_make_buffer_rsrc(o, 0, 2048 * 448 * 2, 0x00020000);
    const bool top = aa.a[0] != aa.a[1];
#pragma unroll
    for (int hp = 0; hp < 2; ++hp) {
        const unsigned p01 = pk2(l[hp][0] * sm[hp], l[hp][1] * sm[hp]), p23 = pk2(l[hp][2] * sm[hp], l[hp][3] * sm[hp]);
        auto e01 = __builtin_amdgcn_permlane32_swap(p01, p01, false, false);
        auto e23 = __builtin_amdgcn_permlane32_swap(p23, p23, false, false);
        u32x4 ov[2];
#pragma unroll
        for (int e = 0; e < 2; ++e) {
            const int hh = 2 * hp + e;
            u32x4 pw = {e01[e] & m0[0], e23[e] & m0[1], e01[e] & m1[0], e23[e] & m1[1]};
            const bf16x8 pf = __builtin_bit_cast(bf16x8, pw);
            const f32x4 o0 = MFMA(v0[hh], pf, z), o1 = MFMA(v1[hh], pf, z);
            ov[e] = u32x4{pk2(o0[0], o0[1]), pk2(o0[2], o0[3]), pk2(o1[0], o1[1]), pk2(o1[2], o1[3])};
        }
        ov[0][2] = top ? ov[1][0] : ov[0][2]; ov[0][3] = top ? ov[1][1] : ov[0][3];
        __builtin_amdgcn_raw_buffer_store_b128(ov[0], rs_o, aa.b[1], hp * 7 * 2048, 16);
        if (!top) __builtin_amdgcn_raw_buffer_store_b128(ov[1], rs_o, aa.b[1], (hp * 7 + 4) * 2048, 16);
    }
    FTA(4);
    if (w >= 4) {
        int l2 = lane;
        asm volatile("" : "+v"(l2));
        __builtin_assume(l2 >= 0 && l2 < 64);
        wo_init(wq, l2, c);
#pragma unroll 1
        for (int t = 0; t < WE; ++t) wo_req(wq, smem + W_A + t * 4096 + c * 1024);
    } else wait_vm<0>();
    const unsigned hv3 = hs_take<WE>(hv2);
    FTA(5);
    BARL();
    return hv3;
}
__device__ __forceinline__ void oproj(char* smem, u16* __restrict__ o, u16* __restrict__ out, int rt, int rg, int ct, int lane) {
    const int fr = lane & 15, fq = lane >> 4;
    const __amdgpu_buffer_rsrc_t rs_o = __builtin_amdgcn_make_buffer_rsrc(o, 0, 2048 * 448 * 2, 0x00020000);
    const unsigned ga = (unsigned)(rt * 56 + fq) * 2048u + (unsigned)(rg * 16 + fr) * 16u;
    bf16x8 fa[14];
#pragma unroll
    for (int ks = 0; ks < 14; ++ks) fa[ks] = __builtin_bit_cast(bf16x8, __builtin_amdgcn_raw_buffer_load_b128(rs_o, ga + ks % 7 * 8192u, ks / 7 * 57344, 16));
    SB();
    const int q = (lane >> 2) & 3, p = lane & 3, s = 2 * (fq & 1) + (q >> 1);
    const char* aw[4];
#pragma unroll
    for (int c = 0; c < 4; ++c) aw[c] = smem + (8 * fq + q) * 128 + ((c ^ s) * 32) + 8 * p;
    f32x4 acc[4];
#pragma unroll
    for (int c = 0; c < 4; ++c) acc[c] = f32x4{0.f, 0.f, 0.f, 0.f};
    constexpr int OA = 9;
    bf16x8 fb[14][4];
#pragma unroll
    for (int ks = 0; ks < 14 + OA; ++ks) {
        if (ks >= OA) {
#pragma unroll
            for (int c = 0; c < 4; ++c) acc[c] = MFMA(fb[ks - OA][c], fa[ks - OA], acc[c]);
        }
        if (ks < 14) {
#pragma unroll
            for (int c = 0; c < 4; ++c) fb[ks][c] = cat(lds_tr(aw[c] + ks * 4096), lds_tr(aw[c] + ks * 4096 + 512));
        }
        SB();
    }
    STAMP(1, 3);
    const __amdgpu_buffer_rsrc_t rs_out = __builtin_amdgcn_make_buffer_rsrc(out, 0, 2048 * 896 * 2, 0x00020000);
    const unsigned ob = (unsigned)((rt * 128 + (rg >> 1) * 32 + 2 * (fr & 7) + (rg & 1)) * 896 + ct * 64 + (fr & 8) * 4 + (fq & 1) * 16 + (fq >> 1) * 8) * 2u;
    u32x4 v[2];
#pragma unroll
    for (int pr = 0; pr < 2; ++pr) {
        const f32x4 a = acc[2 * pr], b = acc[2 * pr + 1];
        auto x = __builtin_amdgcn_permlane16_swap(pk2(a[0], a[1]), pk2(b[0], b[1]), false, false);
        auto y = __builtin_amdgcn_permlane16_swap(pk2(a[2], a[3]), pk2(b[2], b[3]), false, false);
        v[pr] = u32x4{x[0], y[0], x[1], y[1]};
    }
    u32x4 p0, p1;
#pragma unroll
    for (int i = 0; i < 4; ++i) {
        p0[i] = __builtin_amdgcn_update_dpp(v[0][i], v[1][i], 0x128, 0xf, 0xc, false);
        p1[i] = __builtin_amdgcn_update_dpp(v[1][i], v[0][i], 0x128, 0xf, 0x3, false);
    }
    st16_out<16>(rs_out, ob, p0);
    st16_out<16>(rs_out, ob + 16 * 1792u, p1);
}

__global__ __launch_bounds__(512, 2) void kf(const u16* xA, const u16* wqkv, const char* xB, const char* Hr, const char* Hi, const char* Cr,
                                          const char* Ci, const char* wo, u16* out, char* ws) {
    __shared__ __attribute__((aligned(16))) char smem[LDS_TOTAL];
    int tid = threadIdx.x, bid = blockIdx.x;
    u64 pc0;
    {
        unsigned part, off;
        asm volatile("s_getpc_b64 %0\n\ts_bfe_u32 %1, %2, 0x30003\n\ts_mulk_i32 %1, %3" : "=&s"(pc0), "=&s"(part), "+s"(bid) : "i"(CODE_PART) : "scc");
        asm volatile("s_mov_b32 m0, %4\n\tv_mad_u32_u24 %0, %1, 12, %2\n\tglobal_load_lds_dword %0, %3" : "=&v"(off), "+v"(tid) : "s"(part), "s"(pc0), "i"(Q_DUMMY) : "memory");
        asm volatile("" : "+s"(bid));
    }
    __builtin_assume(tid >= 0 && tid < 512 && bid >= 0 && bid < 256);
    const int lane = tid & 63, xcd = bid & 7;
    int w = __builtin_amdgcn_readfirstlane(tid >> 6), j = bid >> 3;
    STAMP(0, 0);
    const int ks = xcd & 3, rl = j & 3, c6 = j >> 2, r8 = (xcd >> 2) * 4 + rl;
    BL q;
    if (j < 24 && w < 4) {
        unsigned ox = (unsigned)(((ks & 1) * 1024 + r8 * 64) * 896), oh = (unsigned)((c6 < 5 ? c6 * 80 : 384) * 896);
#define KR 1
        q.kc = c6 + KR * rl >= 7 ? c6 + KR * rl - 7 : c6 + KR * rl;
        b_off(q, c6 < 4 ? 79 : 63, lane, w);
        const char *p0 = xB, *p1 = Hr, *p2 = Hi;
        asm volatile("" : "+v"(q.offX), "+v"(q.offH[0]), "+v"(q.offH[1]), "+v"(q.offH[2]));
        asm volatile("" : "+s"(p0), "+s"(p1), "+s"(p2), "+s"(ox), "+s"(oh), "+s"(q.kc));
        q.xg = p0 + ox;
        q.hg = ((ks >> 1) ? p2 : p1) + oh;
        STAMP(0, 7);
        reqH(q, smem, w, 0); reqX(q, smem, w, 0);
        reqH(q, smem, w, 1); reqX(q, smem, w, 1);
        asm volatile("" ::"s"(xA), "s"(wqkv), "s"(Cr), "s"(Ci), "s"(wo), "s"(out), "s"(ws));
    }
    asm volatile("" : "+s"(w), "+s"(j));
    __builtin_assume(w >= 0 && w < 8 && j >= 0 && j < 32);
    if (w == 7) *reinterpret_cast<u32x2*>(smem + L_GO) = u32x2{0u, 0u};
    Hs hs;
    hs.base = (const unsigned*)(ws + WS_HS); hs.off = 0; hs.snap = 0;
    int ro = 0, rg = 0, ct = 0;
    bool duty = true;
    u64 again = 0, mr = M_R14;
    unsigned ka = 0, kb = 0;
    int wb = W_0;
    if (j < 24) {
        u16* ab = (u16*)ws;
        u16* px = (u16*)(ws + WS_PX);
        const int rbl = j & 7, cg = j >> 3, rb = (xcd >> 2) * 8 + rbl;
#define DU 1
#define U2 0
        duty = DU ? (j & 19) != 3 : j >= 4;
        const int un = DU ? j - min(c6, 4) : j - 4, uc = un >= 14 ? un - 14 : un;
        ro = xcd * 2 + (un >= 14); ct = (uc >> 1) & 7; rg = (uc & 1) * 4 + (w & 3);
        const int g1 = ks * 3 + cg;
        if (w < 4) {
            const char* cgp = ((ks >> 1) ? Ci : Cr) + (long)rb * (128 * 4096) + (ks & 1) * 2048;
            b_load(smem, q, cgp, (const char*)ab + g1 * (16 * B_SL), (u64)((0x76543210u + rbl * 0x11111111u) & 0x77777777u) << 32 |
                   (((cg == 0 ? TC0 : cg == 1 ? TC1 : TC2) + rbl * 0x11111111u) & 0x77777777u), ks, cg == 2,
                   wo + (duty ? ct : 0) * 128, duty, hs, lane, w, r8 * 2 + (c6 & 1),
                   pc0 + (unsigned)min(j * CODE_LN, (int)CODE_MAX / 128 - CODE_LN) * 128u);
        } else {
            const int cw = w - 4;
            asm volatile("" ::"s"(xB), "s"(Hr), "s"(Hi));
            hs.off = hs_off(lane, g1 * 256, HS_X + ((rb * 3 + cg) * 4 + cw) * 256, ro, uc & 15);
            unsigned v1 = 0;
            hs_rmw(hs, v1, w == 5 ? M_LO | M_HI | M_XW | M_XR | M_OW : M_XW | M_XR);
            STAMP(0, 7);
            auto post = [&]() {
                if (w == 5) *reinterpret_cast<u32x2*>(smem + L_KEY + lane * 8) = u32x2{hs.off, hs_take<0>(v1)};
            };
            const unsigned dbase = (unsigned)((g1 * 16 + r8 * 2) * B_SL + (c6 & 1) * (c6 < 4 ? 80 : 64) * 64);
            const unsigned sg = (ks >> 1) ? 0x80008000u : 0u;
            if (c6 < 4 && !(cw & 1)) ab_mma<3>(smem, dbase, sg, lane, cw >> 1, 0, ab, post);
            else ab_mma<2>(smem, dbase, sg, lane, cw >> 1, (cw & 1) * (c6 < 4 ? 3 : 2), ab, post);
            hs.snap = hs_take<63>(v1);
            hs_add(hs, 1ull << (36 + ks));
            const unsigned pxb = (unsigned)(rb * 3 + cg) * 131072u, ob0 = (unsigned)((rb * 128 + ks * 16) * 896 + 448 + cg * 160) * 2u;
            if (cg == 2) y_mma<4>(smem, lane, cw, px, pxb, ks, out, ob0, hs);
            else y_mma<5>(smem, lane, cw, px, pxb, ks, out, ob0, hs);
            duty = false;
        }
    } else {
        const int qi = j - 24;
#define AM 1
#define HGM 0x3102
        const int hg = HGM >> (4 * ks) & 3, rt = (xcd >> 2) * 8 + qi;
#define XT4 0x5401
        int kst = qi + (XT4 >> (4 * hg) & 15); kst = kst >= 7 ? kst - 7 : kst;
        ro = xcd * 2 + 1; ct = 3 + (qi >> 1); rg = (qi & 1) * 4 + (w & 3); duty = w < 4;
        const int co = 6 + qi;
        WO wq;
        wq.g = wo + ct * 128;
        const unsigned hv = qkv_attn(smem, xA, wqkv, wo, ws, rt, hg, tid, w, hs, kst, ro, co,
                                     (u64)(((ks >> 1) ? Ci : Cr) + (long)((xcd >> 2) * 8 + qi) * (128 * 4096) + (ks & 1) * 2048), qi, wq);
        STAMP(0, 21);
        if (w == 0) {
            hs_rmw(hs, ka, 1ull << hg | M_OW, lane == hg);
            if (!hs_new(hs, hv, mr)) again = 1ull << hg;
        }
        if (w >= 4) {
#pragma unroll 1
            for (int t = WE; t < 14; ++t) wo_req(wq, smem + W_A + t * 4096 + (w & 3) * 1024);
            wait_vm<0>();
        }
        BARL();
        wb = W_A;
    }
    if (duty) {
        volatile LDS_AS unsigned* go = (volatile LDS_AS unsigned*)(smem + L_GO);
        if (w == 0) { if (j < 24) { hs_rmw(hs, ka, M_OW); __builtin_amdgcn_s_sleep(PD2); } hs_wait2(hs, M_OW, M_OW, 0u, ka, kb); *go = 1u; }
        else lds_wait<SL2>(L_GO);
        asm volatile("" ::: "memory");
        STAMP(1, 0);
        if (w == 0) hs_add(hs, M_OR);
        oproj(smem + wb, (u16*)(ws + WS_O), out, ro, rg, ct, lane);
        hs_keep(ka, kb);
        if (again) { hs_wait(hs, mr, mr, 0u); hs_add(hs, again); }
    }
    STAMP(1, 24);
}

extern "C" void kernel_launch(void* const* d_in, const int* in_sizes, int n_in, void* d_out, int out_size,
                              void* d_ws, size_t ws_size, hipStream_t stream) {
    kf<<<256, 512, 0, stream>>>((const u16*)d_in[0], (const u16*)d_in[1], (const char*)d_in[3], (const char*)d_in[6], (const char*)d_in[7],
                                (const char*)d_in[4], (const char*)d_in[5], (const char*)d_in[2], (u16*)d_out, (char*)d_ws);
}
```

```cpp
#include <hip/hip_runtime.h>
#include <hip/hip_bf16.h>

typedef unsigned short u16;
typedef __attribute__((ext_vector_type(8))) short bf16x8;
typedef __attribute__((ext_vector_type(4))) short s16x4;
typedef __attribute__((ext_vector_type(4))) float f32x4;
typedef __attribute__((ext_vector_type(2))) __bf16 bf2;
typedef __attribute__((ext_vector_type(2))) unsigned u32x2;
typedef __attribute__((ext_vector_type(4))) unsigned u32x4;

#define LDS_AS __attribute__((address_space(3)))
#define GLB_AS __attribute__((address_space(1)))

constexpr int D = 448;

#define STAMP(kern, idx)
#define STAMPC(kern, idx)

typedef __attribute__((ext_vector_type(2))) float f32x2;
__device__ __forceinline__ unsigned pk2(float a, float b) {
    f32x2 v = {a, b};
    return __builtin_bit_cast(unsigned, __builtin_convertvector(v, bf2));
}
__device__ __forceinline__ void glds16a(const void* sbase, unsigned voff, const char* l) {
    const unsigned la = (unsigned)(size_t)(LDS_AS const char*)l;
    asm volatile("s_mov_b32 m0, %2\n\ts_nop 0\n\tglobal_load_lds_dwordx4 %0, %1" ::"v"(voff), "s"(sbase), "s"(la) : "memory");
}
__device__ __forceinline__ float vmax(float a, float b) { return __builtin_elementwise_maximum(a, b); }
__device__ __forceinline__ float vmax3(float a, float b, float c) { return vmax(vmax(a, b), c); }
__device__ __forceinline__ bf16x8 lds_rd128(const char* p) { return *reinterpret_cast<const bf16x8*>(p); }
__device__ __forceinline__ s16x4 lds_tr(const char* p) {
    return __builtin_amdgcn_ds_read_tr16_b64_v4i16((LDS_AS s16x4*)p);
}
__device__ __forceinline__ bf16x8 cat(s16x4 a, s16x4 b) {
    bf16x8 r;
    r[0] = a[0]; r[1] = a[1]; r[2] = a[2]; r[3] = a[3];
    r[4] = b[0]; r[5] = b[1]; r[6] = b[2]; r[7] = b[3];
    return r;
}
#define MFMA(a, b, c) __builtin_amdgcn_mfma_f32_16x16x32_bf16((a), (b), (c), 0, 0, 0)
template <int N> __device__ __forceinline__ void wait_vm() { asm volatile("s_waitcnt vmcnt(%0)" ::"i"(N) : "memory"); }
#define SB() __builtin_amdgcn_sched_barrier(0)
template <int NP, int NM> __device__ __forceinline__ void ilv() {
#pragma unroll
    for (int i = 0; i < NP; ++i) {
        __builtin_amdgcn_sched_group_barrier(0x8, 1, 0);
        __builtin_amdgcn_sched_group_barrier(0x100, 1, 0);
    }
    if constexpr (NM > 0) __builtin_amdgcn_sched_group_barrier(0x8, NM, 0);
}
#define BAR()                                               \
    do {                                                    \
        asm volatile("s_waitcnt lgkmcnt(0)" ::: "memory");  \
        __builtin_amdgcn_s_barrier();                       \
        asm volatile("" ::: "memory");                      \
    } while (0)
#define BARL()                                              \
    do {                                                    \
        asm volatile("" ::: "memory");                      \
        __builtin_amdgcn_s_barrier();                       \
        asm volatile("" ::: "memory");                      \
    } while (0)

#define BARK() do { __builtin_amdgcn_s_waitcnt(0xc07f); asm volatile("" ::: "memory"); __builtin_amdgcn_s_barrier(); asm volatile("" ::: "memory"); SB(); } while (0)
__device__ __forceinline__ int kperm(int r) { return (r & ~12) | ((r & 4) << 1) | ((r & 8) >> 1); }

constexpr int LDS_TOTAL = 163840;
constexpr int WS_O = 4 << 20, WS_PX = 8 << 20, WS_HS = 16 << 20;
#define CODEMAX 41984u
constexpr unsigned CODE_MAX = CODEMAX;

typedef unsigned long long u64;
struct Hs {
    const unsigned* base;
    unsigned off, snap;
};
constexpr int HS_O = 16 * 256, HS_X = 32 * 256;
constexpr u64 M_LO = 0xffffull, M_HI = 0xffff0000ull, M_OW = 0xfull << 48, M_OR = 1ull << 52, M_R14 = 0x3fffull << 16;
constexpr u64 M_XW = 0xfull << 32, M_XR = 0xf0ull << 32;
__device__ __forceinline__ unsigned hs_off(int lane, int b, int bx, int ro, int co) {
    const int bo = HS_O + ro * 256 - 48 * 4;
    return (unsigned)(lane * 4 + (lane < 32 ? b : lane < 48 ? bx - 32 * 4 : lane == 52 ? bo + 48 + co * 4 : bo));
}
template <int N> __device__ __forceinline__ unsigned hs_take(unsigned v) {
    unsigned r;
    asm volatile("s_waitcnt vmcnt(%2)\n\tv_mov_b32 %0, %1" : "=v"(r) : "v"(v), "i"(N) : "memory");
    return r;
}
__device__ __forceinline__ void hs_rmw(const Hs& h, unsigned& v, u64 m, unsigned d = 0u) {
    asm volatile("s_mov_b64 exec, %4\n\tglobal_atomic_add %0, %1, %2, %3 sc0 sc1\n\ts_mov_b64 exec, -1" : "+v"(v) : "v"(h.off), "v"(d), "s"(h.base), "s"(m) : "memory");
}
__device__ __forceinline__ void hs_add(const Hs& h, u64 m) {
    const unsigned one = 1u;
    asm volatile("s_mov_b64 exec, %3\n\tglobal_atomic_add %0, %1, %2 sc1\n\ts_mov_b64 exec, -1" ::"v"(h.off), "v"(one), "s"(h.base), "s"(m) : "memory");
}
__device__ __forceinline__ bool hs_new(const Hs& h, unsigned v, u64 m) { return (__ballot(v == h.snap) & m) == 0; }
#define PSL 2
__device__ __forceinline__ void hs_wait(const Hs& h, u64 m, u64 e, unsigned d) {
#pragma unroll 1
    for (unsigned spin = 0; spin < 40000u; ++spin) {
        unsigned v = 0;
        hs_rmw(h, v, e, d);
        if (hs_new(h, hs_take<0>(v), m)) return;
        __builtin_amdgcn_s_sleep(PSL);
    }
}
#define PD2 5
__device__ __forceinline__ void hs_wait2(const Hs& h, u64 m, u64 e, unsigned d, unsigned& a, unsigned& b) {
    unsigned n = 40000u;
    asm volatile("s_mov_b64 exec, %8\n"
                 "1:\n\tglobal_atomic_add %1, %3, %4, %5 sc0 sc1\n\ts_waitcnt vmcnt(1)\n\tv_cmp_eq_u32 vcc, %0, %6\n\ts_and_b64 vcc, vcc, %7\n\ts_cbranch_scc0 2f\n\t"
                 "global_atomic_add %0, %3, %4, %5 sc0 sc1\n\ts_waitcnt vmcnt(1)\n\tv_cmp_eq_u32 vcc, %1, %6\n\ts_and_b64 vcc, vcc, %7\n\ts_cbranch_scc0 2f\n\t"
                 "s_sub_u32 %2, %2, 1\n\ts_cbranch_scc0 1b\n"
                 "2:\n\ts_mov_b64 exec, -1"
                 : "+&v"(a), "+&v"(b), "+&s"(n) : "v"(h.off), "v"(d), "s"(h.base), "v"(h.snap), "s"(m), "s"(e) : "memory", "vcc", "scc");
}
__device__ __forceinline__ void hs_keep(unsigned a, unsigned b) { asm volatile("s_waitcnt vmcnt(0)" ::"v"(a), "v"(b) : "memory"); }
template <int POL> __device__ __forceinline__ void st16_out(__amdgpu_buffer_rsrc_t r, unsigned off, u32x4 v) {
    __builtin_amdgcn_raw_buffer_store_b128(v, r, off, 0, POL);
}
__device__ __forceinline__ void st16_sc1(__amdgpu_buffer_rsrc_t r, unsigned off, u32x4 v) {
    __builtin_amdgcn_raw_buffer_store_b128(v, r, off, 0, 16);
}
__device__ __forceinline__ void glds16m(const void* sbase, unsigned voff, const char* l, u64 mask) {
    const unsigned la = (unsigned)(size_t)(LDS_AS const char*)l;
    asm volatile("s_mov_b32 m0, %2\n\ts_mov_b64 exec, %3\n\tglobal_load_lds_dwordx4 %0, %1\n\ts_mov_b64 exec, -1" ::"v"(voff), "s"(sbase), "s"(la), "s"(mask) : "memory");
}
__device__ __forceinline__ void glds16c(const void* sbase, unsigned voff, const char* l) {
    const unsigned la = (unsigned)(size_t)(LDS_AS const char*)l;
    asm volatile("s_mov_b32 m0, %2\n\ts_nop 0\n\tglobal_load_lds_dwordx4 %0, %1 sc1" ::"v"(voff), "s"(sbase), "s"(la) : "memory");
}
__device__ __forceinline__ void glds16mc(const void* sbase, unsigned voff, const char* l, u64 mask) {
    const unsigned la = (unsigned)(size_t)(LDS_AS const char*)l;
    asm volatile("s_mov_b32 m0, %2\n\ts_mov_b64 exec, %3\n\tglobal_load_lds_dwordx4 %0, %1 sc1\n\ts_mov_b64 exec, -1" ::"v"(voff), "s"(sbase), "s"(la), "s"(mask) : "memory");
}
template <int SL> __device__ __forceinline__ void lds_wait(unsigned a) {
    unsigned n = 1000000u, v;
    asm volatile("1:\n\ts_sleep %3\n\tds_read_b32 %0, %2\n\ts_sub_u32 %1, %1, 1\n\ts_cbranch_scc1 2f\n\ts_waitcnt lgkmcnt(0)\n\tv_cmp_eq_u32 vcc, 0, %0\n\ts_cbranch_vccnz 1b\n"
                 "2:\n\ts_waitcnt lgkmcnt(0)" : "=&v"(v), "+s"(n) : "v"(a), "i"(SL) : "memory", "vcc", "scc");
}

constexpr int A_SL = 16384, B_R0 = 6 * A_SL, B_SL = 20480, X_SL = 8192, H_SL = 10240, H_0 = 11 * X_SL, L_KEY = H_0 + 7 * H_SL, L_GO = 163328, W_0 = A_SL;
constexpr int CODE_PART = (CODE_MAX - 6144) / 7 / 128 * 128;
#define TT(i)
struct WO { const char* g; u64 pat; unsigned off, hi; };
__device__ __forceinline__ void wo_init(WO& q, int lane, int w) {
    q.g += w * (8 * 896);
    q.pat = 0ull;
#pragma unroll
    for (int i = 0; i < 21; ++i) q.pat |= (u64)(i % 7 == 6 ? 7 : (i % 7 + 1) / 2) << (3 * i);
    q.pat >>= 6 * w;
    q.hi = (unsigned)(lane >> 5) * (3 * 896);
    q.off = (unsigned)(((lane >> 5) * 16 + ((lane >> 3) & 3)) * 896 + ((lane & 7) ^ ((w & 1) * 4 + ((lane >> 4) & 1) * 2)) * 16);
    asm volatile("" : "+v"(q.off), "+v"(q.hi));
}
__device__ __forceinline__ void wo_req(WO& q, const char* l) {
    glds16a(q.g - ((unsigned)q.pat & 3) * (4 * 896), q.off + ((unsigned)q.pat & 4) * q.hi, l);
    q.g += 32 * 896; q.pat >>= 3;
}
#define SL1 1
#define SL2 1
#define N4 3
#define N5 4
#define N6 5
#define KA 3
#define V1 1
#define PDG 1
#define DEF 2
#define TC0 0x75643210u
#define TC1 0x67540132u
#define TD0 0x76543210u
#define TD1 TD0
#define TD2 TD0
#define TQ 0x76543210u
#define TC2 0x54763201u
#define LP 2
#define WP1 3
#define WP2 10
#define WE 2
struct BL {
    const char *xg, *hg;
    unsigned offX, offH[3];
    int kc;
};
__device__ __forceinline__ void reqX(BL& q, char* smem, int w, int sx) {
    const char* gx = q.xg + q.kc * 128;
    const char* la = smem + sx * X_SL + w * 2048;
    glds16a(gx, q.offX, la);
    glds16a(gx + 8 * 896, q.offX, la + 1024);
    q.kc = q.kc == 6 ? 0 : q.kc + 1;
}
__device__ __forceinline__ void reqH(BL& q, char* smem, int w, int k) {
    const char* gh = q.hg + q.kc * 128;
    const char* lh = smem + H_0 + k * H_SL + w * 2560;
    glds16a(gh, q.offH[0], lh);
    glds16a(gh, q.offH[1], lh + 1024);
    glds16m(gh, q.offH[2], lh + 2048, 0xffffffffull);
}
__device__ __forceinline__ void b_off(BL& q, int hn, int lane, int w) {
    const int r8 = lane >> 3, c = (lane & 7) ^ r8;
    q.offX = (unsigned)((w * 16 + r8) * 896 + c * 16);
#pragma unroll
    for (int i = 0; i < 3; ++i) q.offH[i] = (unsigned)(min(w * 20 + i * 8 + r8, hn) * 896 + (c ^ ((w & 1) * 4)) * 16);
}
__device__ __forceinline__ void b_load(char* smem, BL& q, const char* cg, const char* abg, u64 tb, int ks,
                                       bool narrow, const char* wog, bool duty, Hs& hs, int lane, int w, int pm) {
    const int r8 = lane >> 3, c = (lane & 7) ^ r8;
    unsigned offA[4];
#pragma unroll
    for (int i = 0; i < 4; ++i)
        offA[i] = (unsigned)(((w >> 1) * 64 + (((w & 1) * 2 + (i >> 1) + ks) & 3) * 16 + (i & 1) * 8 + r8) * 4096 + c * 16);
    const int rb0 = w * (narrow ? 32 : 40);
    unsigned offB = (unsigned)((c >> 2) * 10240 + (rb0 + r8) * 64 + (c & 3) * 16);
    u64 mB = narrow ? 1ull : ~0ull;
    int sa = 3;
    u64 tc = tb | 0x8888888800000000ull;
    const unsigned t3 = (unsigned)tb;
    auto reqC = [&]() {
        const char* g = cg + ((unsigned)tc & 15) * 128;
        const char* la = smem + sa * A_SL + w * 4096;
#pragma unroll
        for (int i = 0; i < 4; ++i) glds16a(g, offA[i], la + i * 1024);
        sa = sa == 5 ? 0 : sa + 1; tc >>= 4;
    };
    int sb = 0, lb0 = B_R0 + rb0 * 128;
    tb = tb << 1 | 0x1111111100000000ull;
    auto reqB = [&]() {
        const char* g = abg + ((unsigned)tb & 15) * B_SL;
        const char* lb = smem + lb0 + sb * B_SL;
#pragma unroll
        for (int i = 0; i < 4; ++i) glds16c(g + i * 512, offB, lb + i * 1024);
        glds16mc(g + 2048, offB, lb + 4096, mB);
        sb = sb == 2 ? 0 : sb + 1; tb >>= 4;
    };
    auto step = [&]() { reqB(); reqC(); wait_vm<13>(); BARL(); };
    u64 tt[11] = {0, 0, 0, 0, 0, 0, 0, 0, 0, 0, 0};
#pragma unroll 1
    for (int k = 2; k < 7; ++k) {
        reqH(q, smem, w, k); reqX(q, smem, w, k == 6 ? 10 : k);
        wait_vm<10>();
        BARL();
    }
    q.xg += 512 * 896;
    auto reqX2 = [&](int a, int b) {
#pragma unroll
        for (int i = a; i < b; ++i) reqX(q, smem, w, i == 6 ? 10 : i);
    };
    reqX2(0, N4);
    wait_vm<5 + 2 * N4>();
    BARL();
    reqX2(N4, N5);
    wait_vm<2 * N5>();
    BARL();
    TT(0);
    reqX2(N5, N6);
    unsigned own = (lane & 15) == pm, hv = 0, hw = 0;
    u64 m3 = 3u << (t3 << 1 & 14) | 3u << (t3 >> 3 & 14) | 3u << (t3 >> 7 & 14);
    asm volatile("" : "+v"(offA[0]), "+v"(offA[1]), "+v"(offA[2]), "+v"(offA[3]), "+v"(offB), "+v"(own));
    asm volatile("" : "+s"(mB), "+s"(m3), "+s"(abg), "+s"(cg), "+s"(lb0), "+s"(tb), "+s"(tc));
    if (w == 0) {
        const u32x2 t = *reinterpret_cast<const u32x2*>(smem + L_KEY + lane * 8);
        hs.off = t[0]; hs.snap = t[1];
    }
    wait_vm<2 * (N6 - KA)>();
    BARL();
    TT(1);
    reqX2(N6, 7);
    reqC();
    if (V1) reqC();
    wait_vm<4 + 4 * V1>();
    BARL();
    TT(2);
    if (w == 0) hs_add(hs, 1ull << pm);
    if (!V1) reqC();
#define PDA 5
    if (PDA && w == 0) { __builtin_amdgcn_s_sleep(PDA); hs_rmw(hs, hv, M_LO); }
    BARL();
    TT(3);
    if (w == 0) {
        if (!PDA) hs_rmw(hs, hv, M_LO);
        __builtin_amdgcn_s_sleep(PDG);
        hs_wait2(hs, m3, M_LO, own, hv, hw);
        *(volatile LDS_AS unsigned*)(smem + L_GO + 4) = 1u;
    } else lds_wait<SL1>(L_GO + 4);
    TT(4);
    reqB(); reqB();
    unsigned h2 = 0;
    if (w == 0) hs_rmw(hs, h2, M_LO);
    reqC();
    wait_vm<9>();
    asm volatile("" ::"v"(hv), "v"(hw));
    BARL();
    reqB(); reqC();
    {
        const unsigned v = hs_take<13>(h2);
        if (w == 0) { if (!hs_new(hs, v, M_LO)) hs_wait(hs, M_LO, M_LO, own); }
    }
    BARL();
    if (w == 0) hs_add(hs, 0x10001ull << pm | (duty ? M_OR : 0ull));
#pragma unroll 1
    for (int i = 1; i < 4; ++i) step();
    if (w == 0) hs_rmw(hs, h2, M_LO | M_HI, own);
    step();
    if (w == 0) { if (!hs_new(hs, hs_take<9>(h2), M_HI)) hs_wait(hs, M_HI, M_HI, 0u); }
#pragma unroll 1
    for (int i = 5; i < 13; ++i) step();
    WO wq;
    wq.g = wog;
    wo_init(wq, lane, w);
    reqB();
    if (duty) {
#pragma unroll
        for (int n = 0; n < 14; ++n) {
            if (n == WP1) { wait_vm<9 + WP1>(); BARL(); }
            if (n == WP2) { wait_vm<WP2>(); BARL(); }
            wo_req(wq, smem + W_0 + n * 4096 + w * 1024);
        }
    } else {
        wait_vm<9>(); BARL();
        wait_vm<0>(); BARL();
    }
    wait_vm<0>();
    BARL();
}
template <int NE, class F>
__device__ __forceinline__ void ab_mma(char* smem, unsigned dbase, unsigned sg, int lane, int sh, int ef, u16* ab, F post) {
    const int fr = lane & 15, fq = lane >> 4;
    const int z0 = fr * 128 + ((fq ^ (fr & 7)) * 16), z1 = z0 ^ 64;
    unsigned a0 = sh * 4096 + z0, a1 = sh * 4096 + z1, b0 = H_0 + ef * 2048 + z0, b1 = H_0 + ef * 2048 + z1;
    unsigned doff = dbase + (unsigned)(sh * 10240 + (ef * 16 + fr) * 64 + ((fq & 1) * 2 + (fq >> 1)) * 16);
    asm volatile("" : "+v"(a0), "+v"(a1), "+v"(b0), "+v"(b1), "+v"(doff));
    const char *sa0 = smem + a0, *sa1 = smem + a1, *sb0 = smem + b0, *sb1 = smem + b1;
    const __amdgpu_buffer_rsrc_t rs = __builtin_amdgcn_make_buffer_rsrc(ab, 0, 192 * B_SL, 0x00020000);
    f32x4 acc[NE][2];
    bf16x8 xf[2][2][2], hf[2][2][NE];
    auto rd = [&](int k, int i) {
        const int h = i >= 2 + NE, n = i - h * (2 + NE);
        if (n < 2) xf[k & 1][h][n] = lds_rd128((h ? sa1 : sa0) + (k == 6 ? 10 : k) * X_SL + n * 2048);
        else hf[k & 1][h][n - 2] = lds_rd128((h ? sb1 : sb0) + k * H_SL + (n - 2) * 2048);
    };
#pragma unroll 1
    for (int p = 0; p < 2; ++p) {
#pragma unroll
        for (int a = 0; a < NE; ++a) {
            acc[a][0] = f32x4{0.f, 0.f, 0.f, 0.f}; acc[a][1] = f32x4{0.f, 0.f, 0.f, 0.f};
            asm volatile("" : "+v"(acc[a][0]), "+v"(acc[a][1]));
        }
#pragma unroll
        for (int k = 0; k < 8; ++k) {
            if (k == 0) BARL();
            else if (k == KA) { if (p) wait_vm<0>(); BARL(); }
            else if (k == 7) { if (p) BAR(); }
            else if (!p) { if (k == 6) { post(); BAR(); } else BARL(); }
            if (k == 0 && p == 0) STAMP(0, 1);
            SB();
#pragma unroll
            for (int i = 0; i < 4 * NE; ++i) {
                const int h = i >= 2 * NE, n = i - h * 2 * NE;
                if (k == 7) acc[n >> 1][n & 1] = MFMA(xf[0][h][n & 1], hf[0][h][n >> 1], acc[n >> 1][n & 1]);
                else if (k) asm volatile("v_mfma_f32_16x16x32_bf16 %0, %1, %2, %0" : "+v"(acc[n >> 1][n & 1]) : "v"(xf[~k & 1][h][n & 1]), "v"(hf[~k & 1][h][n >> 1]));
                SB();
                if (k < 7 && i < 4 + 2 * NE) rd(k, i);
                SB();
            }
        }
#pragma unroll
        for (int mi = 0; mi < NE; ++mi) {
            const f32x4 a = acc[mi][0], b = acc[mi][1];
            auto x = __builtin_amdgcn_permlane16_swap(pk2(a[0], a[1]) ^ sg, pk2(b[0], b[1]) ^ sg, false, false);
            auto y = __builtin_amdgcn_permlane16_swap(pk2(a[2], a[3]) ^ sg, pk2(b[2], b[3]) ^ sg, false, false);
            st16_sc1(rs, doff + mi * 1024u, u32x4{x[0], y[0], x[1], y[1]});
        }
        SB();
        doff += B_SL;
    }
}
template <int NF>
__device__ __forceinline__ void y_mma(char* smem, int lane, int cw, u16* px, unsigned pxb, int ks, u16* out, unsigned ob0, Hs& hs) {
    const int fr = lane & 15, fq = lane >> 4;
    const int mh = cw >> 1, nh = cw & 1;
    f32x4 acc[5][4];
#pragma unroll
    for (int a = 0; a < NF; ++a)
#pragma unroll
        for (int b = 0; b < 4; ++b) { acc[a][b] = f32x4{0.f, 0.f, 0.f, 0.f}; asm volatile("" : "+v"(acc[a][b])); }
    const int z0 = fr * 128 + ((fq ^ (fr & 7)) * 16), z1 = z0 ^ 64;
    unsigned a0 = 3 * A_SL + mh * 8192 + z0, a1 = 3 * A_SL + mh * 8192 + z1, b0 = B_R0 + nh * (NF * 2048) + z0, b1 = B_R0 + nh * (NF * 2048) + z1;
    unsigned pl = pxb + (unsigned)(cw * 8192 + lane * 16), ph = pl + (unsigned)(6144 - 512 - lane * 8);
    if (NF == 5) asm volatile("" : "+v"(ph));
    unsigned ob = ob0 + (unsigned)((mh * 64 + fr) * 896 + nh * (NF * 16) + (fq & 1) * 16 + (fq >> 1) * 8) * 2u;
    const unsigned one = fq == (fr >> 2) ? 0x3f80u << ((fr & 1) * 16) : 0u, d0 = (fr & 2) ? 0u : one, d1 = (fr & 2) ? one : 0u;
    u32x4 s0 = {d0, d1, 0u, 0u}, s1 = {0u, 0u, d0, d1};
    unsigned own = lane == 32 + ks;
    asm volatile("" : "+v"(a0), "+v"(a1), "+v"(b0), "+v"(b1), "+v"(pl), "+v"(ob), "+v"(s0), "+v"(s1), "+v"(own));
    const char *sa0 = smem + a0, *sa1 = smem + a1, *sb0 = smem + b0, *sb1 = smem + b1;
    const __amdgpu_buffer_rsrc_t rs = __builtin_amdgcn_make_buffer_rsrc(px, 0, 48 * 4 * 32768, 0x00020000);
    const __amdgpu_buffer_rsrc_t rs_out = __builtin_amdgcn_make_buffer_rsrc(out, 0, 2048 * 896 * 2, 0x00020000);
    bf16x8 cf[2][4], xf[2][NF];
    auto rdx = [&](int mi, int k, int st) { xf[k][mi] = lds_rd128((k ? sb1 : sb0) + st * B_SL + mi * 2048); };
    auto rdc = [&](int l, int k, int st) { cf[k][l] = lds_rd128((k ? sa1 : sa0) + st * A_SL + l * 2048); };
    auto mf = [&](int mi, int l, int k) {
        asm volatile("v_mfma_f32_16x16x32_bf16 %0, %1, %2, %0" : "+v"(acc[mi][l]) : "v"(cf[k][l]), "v"(xf[k][mi]));
        SB();
    };
    int da = -3 * A_SL;
    auto head = [&](int st, int lo) {
        int n = 0;
        __builtin_amdgcn_s_waitcnt(0xc47f);
        SB();
#pragma unroll
        for (int i = 4 * lo; i < 16; ++i) {
            mf(((i >> 1) ^ (i >> 2)) & 1, i >> 2, (i ^ (i >> 1)) & 1);
            if (n < 2 * NF - 4) rdx(2 + (n >> 1), n & 1, st);
            if (st == 2 && n == 8) sa0 += da;
            if (st == 2 && n == 10) { sa1 += da; da = -da; }
            ++n;
            SB();
        }
        if (NF == 5) {
#pragma unroll
            for (int i = 0; i < 8 - 2 * lo; ++i) mf(2, 3 - (i >> 1), (i ^ (i >> 1)) & 1);
        }
    };
    auto rest = [&](int st, int lo) {
#pragma unroll
        for (int i = 4 * lo; i < 16; ++i) {
            mf(NF - 2 + (((i >> 1) ^ (i >> 2)) & 1), i >> 2, (i ^ (i >> 1)) & 1);
            if ((i & 3) >= 2) rdc(i >> 2, ~i & 1, st);
            else if (i - 4 * lo < 8) rdx((i >> 2) - lo, i & 1, st);
            else if (lo) rdc(0, i & 1, st);
            SB();
        }
    };
    STAMP(0, 4);
    BARL();
    STAMP(0, 5); STAMPC(0, 3);
#pragma unroll
    for (int i = 0; i < 16; ++i) {
        if ((i & 3) >= 2) rdc(i >> 2, i & 1, 0);
        else if (i < 8) rdx(i >> 2, i & 1, 0);
        SB();
    }
#pragma unroll 1
    for (int t = 0; t < 4; ++t) {
        head(0, 0);
        wait_vm<0>();
        BARK();
        rest(1, 0); head(1, 0);
        BARK();
        rest(2, 0); head(2, 0);
        BARK();
        rest(0, 0);
    }
    bf16x8 kf[4];
    unsigned ka = 0, kb = 0;
    head(0, 0); BARK();
    rest(1, 0); head(1, 1); BARK();
    kf[0] = cf[0][0]; kf[1] = cf[1][0];
    rest(2, 1); head(2, 1); BARK();
    hs_rmw(hs, kb, M_XR);
    kf[2] = cf[0][0]; kf[3] = cf[1][0];
    rest(0, 1);
    STAMP(0, 18); STAMPC(0, 4);
    const int pw = ks * 32768;
    auto put = [&](int p, int h) {
        const int sl = 2 * (p - 1) + h;
        const f32x4 a = acc[h < 2 ? 2 * h : NF - 1][p], b = acc[h < 2 ? 2 * h + 1 : 0][p];
        if (h < 2) __builtin_amdgcn_raw_buffer_store_b128(u32x4{pk2(a[0], a[1]), pk2(a[2], a[3]), pk2(b[0], b[1]), pk2(b[2], b[3])}, rs, pl + (sl & 3) * 1024u, pw + (sl >> 2 << 12), 16);
        else __builtin_amdgcn_raw_buffer_store_b64(u32x2{pk2(a[0], a[1]), pk2(a[2], a[3])}, rs, ph + p * 512u, pw, 16);
    };
    constexpr int NG = 2 * NF - 4, NL = 12 + 3 * NG;
#pragma unroll
    for (int i = 0; i < NL + 2 * NF; ++i) {
        const int j = i - 12, l = i < 12 ? 1 + (i >> 2) : i < NL ? 1 + j / NG : 0, mi = i < 12 ? (i >> 1) & 1 : i < NL ? 2 + j % NG / 2 : (i - NL) >> 1;
        acc[mi][l] = MFMA(cf[i & 1][l], xf[i & 1][mi], acc[mi][l]);
        SB();
        if (i < NG) rdx(2 + (i >> 1), i & 1, 0);
        const int e = i - 2;
        if (e == 4 || e == 8 || e == 12) put(e >> 2, 0);
        if (e > 12 && e <= NL && (e - 12) % NG == 4 % NG) put((e - 13) / NG + 1, 1);
        if (NF == 5 && e > 12 && e <= NL && (e - 12) % NG == 0) put((e - 12) / NG, 2);
        if (i > NL + 1) rdx((i - NL - 2) >> 1, i & 1, 1);
        if (i == NL + 3) BARL();
        SB();
    }
    rdx(NF - 1, 0, 1); rdx(NF - 1, 1, 1);
#pragma unroll
    for (int i = 0; i < 4 * NF; ++i) {
        const int mi = (i >> 1) % NF;
        acc[mi][0] = MFMA(kf[(i >= 2 * NF) * 2 + (i & 1)], xf[i & 1][mi], acc[mi][0]);
        SB();
        if (i < 2 * NF) rdx(mi, i & 1, 2);
        SB();
    }
#pragma unroll
    for (int mi = 0; mi < NF; ++mi) asm volatile("" : "+v"(acc[mi][0]));
    STAMP(0, 19);
    const bool all = hs_new(hs, hs_take<0>(kb), M_XR);
    STAMP(0, 20);
    hs_rmw(hs, ka, M_XW, own);
    int pr[4];
#pragma unroll
    for (int l = 1; l < 4; ++l) pr[l - 1] = ((ks - l) & 3) * 32768 + (l == 3 ? 4096 : 0);
    pr[3] = pr[2] - 4096;
    unsigned pl2 = pl, ph2 = ph;
    u32x4 s01 = s0 | s1;
    __amdgpu_buffer_rsrc_t ro2 = rs_out;
    asm volatile("" : "+s"(pr[0]), "+s"(pr[1]), "+s"(pr[2]), "+s"(pr[3]), "+s"(ro2));
    asm volatile("" : "+v"(pl2), "+v"(ph2), "+v"(s01));
    auto fin = [&](int pr) {
        const f32x4 a = acc[2 * pr][0], b = acc[pr < 2 ? 2 * pr + 1 : 2 * pr][0];
        auto x = __builtin_amdgcn_permlane16_swap(pk2(a[0], a[1]), pk2(b[0], b[1]), false, false);
        auto y = __builtin_amdgcn_permlane16_swap(pk2(a[2], a[3]), pk2(b[2], b[3]), false, false);
        if (pr < 2 || !(fq & 1)) st16_out<0>(ro2, ob + pr * 64u, u32x4{x[0], y[0], x[1], y[1]});
        SB();
    };
    u32x2 o5 = {0u, 0u};
    {
        const f32x4 z = {0.f, 0.f, 0.f, 0.f};
#pragma unroll
        for (int h = 0; h < 2; ++h) {
            const f32x4 a = acc[2 * h][0], b = acc[2 * h + 1][0];
            const bf16x8 p = __builtin_bit_cast(bf16x8, u32x4{pk2(a[0], a[1]), pk2(a[2], a[3]), pk2(b[0], b[1]), pk2(b[2], b[3])});
            acc[2 * h][0] = MFMA(p, __builtin_bit_cast(bf16x8, s0), z);
            acc[2 * h + 1][0] = MFMA(p, __builtin_bit_cast(bf16x8, s1), z);
            asm volatile("" : "+v"(acc[2 * h][0]), "+v"(acc[2 * h + 1][0]));
        }
        if (NF == 5) { const f32x4 a = acc[4][0]; o5 = u32x2{pk2(a[0], a[1]), pk2(a[2], a[3])}; acc[4][0] = z; asm volatile("" : "+v"(o5), "+v"(acc[4][0])); }
    }
    __builtin_amdgcn_s_sleep(PD2 - 2);
    hs_wait2(hs, M_XW ^ 1ull << (32 + ks), M_XW ^ 1ull << (32 + ks), 0u, ka, kb);
    STAMP(1, 0);
    bf16x8 pv[2][3];
    u32x2 p5[3];
#pragma unroll
    for (int h = 0; h < (NF + 1) / 2; ++h)
#pragma unroll
        for (int l = 1; l < 4; ++l) {
            const int sl = 2 * (l - 1) + h;
            if (h < 2) pv[h][l - 1] = __builtin_bit_cast(bf16x8, __builtin_amdgcn_raw_buffer_load_b128(rs, pl2 + (sl & 3) * 1024u, pr[l - 1], 16));
            else p5[l - 1] = __builtin_amdgcn_raw_buffer_load_b64(rs, ph2 + l * 512u, pr[l == 3 ? 3 : l - 1], 16);
            SB();
        }
    if (!all) hs_add(hs, 1ull << (32 + ks));
#pragma unroll
    for (int h = 0; h < (NF + 1) / 2; ++h) {
        if (h < 2) {
#pragma unroll
            for (int l = 0; l < 3; ++l) {
                acc[2 * h][0] = MFMA(pv[h][l], __builtin_bit_cast(bf16x8, s0), acc[2 * h][0]);
                acc[2 * h + 1][0] = MFMA(pv[h][l], __builtin_bit_cast(bf16x8, s1), acc[2 * h + 1][0]);
                SB();
                if (h == 1 && l == 1 && NF == 5) fin(0);
            }
        } else {
            acc[4][0] = MFMA(__builtin_bit_cast(bf16x8, (u32x4{p5[0][0], p5[0][1], p5[1][0], p5[1][1]})), __builtin_bit_cast(bf16x8, s01), acc[4][0]);
            acc[4][0] = MFMA(__builtin_bit_cast(bf16x8, (u32x4{p5[2][0], p5[2][1], o5[0], o5[1]})), __builtin_bit_cast(bf16x8, s01), acc[4][0]);
            SB();
        }
    }
    STAMP(1, 1);
    if (NF == 4) {
        u32x4 v[2], p0, p1;
#pragma unroll
        for (int pr = 0; pr < 2; ++pr) {
            const f32x4 a = acc[2 * pr][0], b = acc[2 * pr + 1][0];
            auto x = __builtin_amdgcn_permlane16_swap(pk2(a[0], a[1]), pk2(b[0], b[1]), false, false);
            auto y = __builtin_amdgcn_permlane16_swap(pk2(a[2], a[3]), pk2(b[2], b[3]), false, false);
            v[pr] = u32x4{x[0], y[0], x[1], y[1]};
        }
#pragma unroll
        for (int i = 0; i < 4; ++i) {
            p0[i] = __builtin_amdgcn_update_dpp(v[0][i], v[1][i], 0x128, 0xf, 0xc, false);
            p1[i] = __builtin_amdgcn_update_dpp(v[1][i], v[0][i], 0x128, 0xf, 0x3, false);
        }
        const unsigned o2 = ob - (unsigned)(fr & 8) * (1792 - 8);
        st16_out<16>(ro2, o2, p0);
        st16_out<16>(ro2, o2 + 8 * 1792u, p1);
    } else
    fin(1);
    if (NF == 5) fin(2);
    hs_keep(ka, kb);
}

constexpr int Q_ASL = 18432, Q_WR = 3 * Q_ASL, Q_WSL = 21504, GP = 1216, Q_DUMMY = 162816, W_A = Q_DUMMY - 14 * 4096, Q_TAB = 84 * GP;
__device__ __forceinline__ void qkv_reqW(char* smem, const char* wg, unsigned off, int c, int kw, int sw) {
    const char* g = wg + (long)kw * (32 * 2688);
    const char* sb = smem + Q_WR + sw * Q_WSL + c * 1792;
#pragma unroll
    for (int k = 0; k < 6; ++k) glds16m(g + (k & 1) * (8 * 2688) + (k >> 1) * 896, off, sb + (k & 1) * 896 + (k >> 1) * 7168, 0xffffffffffffffull);
}
struct QX { unsigned off; int r0, r1, r2; };
__device__ __forceinline__ QX qkv_offX(int c, int lane, int s0) {
    QX q;
    q.off = (unsigned)((lane >> 3) * 1792 + ((lane & 7) ^ (lane >> 3)) * 16);
    q.r0 = max(s0 + (c < 2 ? c - 16 : 16 * c - 32), 0) * 896;
    q.r1 = (s0 + 16 * c + 32) * 896;
    q.r2 = q.r1 + (c < 2 ? 64 : -63) * 896;
    return q;
}
__device__ __forceinline__ void qkv_reqX(char* smem, const u16* __restrict__ xA, const QX& q, int c, int kx, int sx) {
    const char* g = (const char*)xA + kx * 128;
    const char* sb = smem + sx * Q_ASL + c * 1024;
    glds16a(g + q.r0, q.off, sb);
    glds16a(g + q.r1, q.off, sb + 4096);
    glds16a(g + q.r2, q.off, sb + 2 * 4096);
    glds16a(g + q.r1 + 896, q.off, sb + 3 * 4096);
    if (c < 2) glds16a(g + q.r1 + 65 * 896, q.off, sb + 4 * 4096);
}
template <int V> struct QS {
    static constexpr int col(int i) { constexpr int t[3][6] = {{0, 1, 2, 5, 3, 4}, {2, 3, 1, 0, 9, 10}, {0, 1, 2, 2, 3, 4}}; return t[V][i]; }
    static constexpr int wof(int i) { return V == 1 ? ((col(i) + 5) / 7) * 7168 + ((col(i) + 5) % 7 - 5) * 32 : col(i) * 32; }
    static constexpr int lo(int i) { return V == 0 ? (i == 3 ? 3 : 1) : V == 1 ? (i == 2 || i == 3 ? 1 : 0) : 0; }
    static constexpr int hi(int i) { return i != 3 || V == 0 ? 9 : V == 1 ? 3 : 0; }
};
#define RS2 0
#define LB 1
#define LC 3
#define LW 4
template <int V>
__device__ __forceinline__ void qkv_mma(char* smem, int lane, int G0, u64 (&tt)[11]) {
    typedef QS<V> S;
    const int fr = lane & 15, fq = lane >> 4;
    const int u = fq & 1, ah = fq >> 1, q = (lane >> 2) & 3, p = lane & 3;
    constexpr int R0 = V == 0 ? 1 : 0;
    f32x4 acc[6][9];
    unsigned aw = Q_WR + Q_WSL + (16 * ah + 4 * u + q) * 224 + 8 * p + (V < 2 ? G0 * 32 : (G0 >> 3) * 7168 + 64);
    unsigned ax0 = fr * 128 + ((fq ^ (fr & 7)) * 16), ax1 = ax0 ^ 64;
    unsigned ai = (G0 * 4 + fq) * GP + fr * 8, ah0 = ai + (fr & 8) * 64;
#pragma unroll
    for (int i = 0; i < 6; ++i)
#pragma unroll
        for (int r = S::lo(i); r < S::hi(i); ++r) { acc[i][r] = f32x4{0.f, 0.f, 0.f, 0.f}; asm volatile("" : "+v"(acc[i][r])); }
    asm volatile("" : "+v"(aw), "+v"(ax0), "+v"(ax1), "+v"(ai));
    if (V) asm volatile("" : "+v"(ah0));
    const char *pw = smem + aw, *px0 = smem + ax0, *px1 = smem + ax1;
    bf16x8 xf[9];
    s16x4 wl[6], wh[6];
    auto rdw = [&](int i, int h) {
        if (S::hi(i) == 0) return;
        if (h) wh[i] = lds_tr(pw + S::wof(i) + 8 * 224); else wl[i] = lds_tr(pw + S::wof(i));
    };
    int cw = 0x2108, cx = 0x24, dw = 0, dx = 0;
    auto tick = [&](int kind, bool extra) {
#pragma unroll
        for (int g = 0; g < 3; ++g) {
            if (g == 2 && (kind == 1 || extra)) BARK();
            int n = 0;
#pragma unroll
            for (int r = 0; r < 9; ++r)
#pragma unroll
                for (int e = 0; e < 2; ++e) {
                    const int i = 2 * g + (e ^ (r & 1));
                    if (r < S::lo(i) || r >= S::hi(i)) { if (g == 2 && e == 1 && r >= R0) { xf[r] = lds_rd128((kind ? px0 : px1) + r * 2048); SB(); } continue; }
                    asm volatile("v_mfma_f32_16x16x32_bf16 %0, %1, %2, %0" : "+v"(acc[i][r]) : "v"(cat(wl[i], wh[i])), "v"(xf[r]));
                    SB();
                    if (g < 2 ? n < 4 : (n < 8 && !(n & 1))) { const int m = g < 2 ? n : n >> 1; rdw(2 * ((g + 1) % 3) + (m >> 1), m & 1); }
                    if (g == 2 && e == 1) xf[r] = lds_rd128((kind ? px0 : px1) + r * 2048);
                    if (g == 0) {
                        if (n == 4) { dw = cw & 1 ? -4 * Q_WSL : Q_WSL; asm volatile("" : "+s"(dw)); }
                        if (n == 6) { cw >>= 1; asm volatile("" : "+s"(cw)); }
                        if (n == 8 && kind) { dx = cx & 1 ? -2 * Q_ASL : Q_ASL; asm volatile("" : "+s"(dx)); }
                        if (n == 10 && kind) { cx >>= 1; asm volatile("" : "+s"(cx)); px0 += dx; }
                        if (n == 12 && kind) px1 += dx;
                    }
                    if (g == 1 && n == 5) pw += dw;
                    ++n;
                    SB();
                }
        }
    };
    auto last = [&]() {
        int fi[48], fw[48], nf = 0, na = 99;
        u32x2 cv[48];
        auto behind = [&](int f) {
            const int a = f - LW, b = f - LC;
            if (a >= 0 && a < nf) {
                const int r = fw[a];
                *reinterpret_cast<u32x2*>(smem + (r ? ai : ah0) + S::col(fi[a]) * 4 * GP + (r == 0 ? 0 : r < 5 ? 128 * r - 64 : 128 * r)) = cv[a];
                SB();
            }
            if (b >= 0 && b < nf) { const f32x4 d = acc[fi[b]][fw[b]]; cv[b] = u32x2{pk2(d[0], d[1]), pk2(d[2], d[3])}; asm volatile("" : "+v"(cv[b])); }
            if (a == na + LB) { SB(); BARK(); }
        };
#pragma unroll
        for (int h = 0; h < (RS2 ? 6 : 3); ++h) {
            const int g = h % 3;
            int n = 0;
            if (h == 3) na = nf - 1;
#pragma unroll
            for (int r = 0; r < 9; ++r)
#pragma unroll
                for (int e = 0; e < 2; ++e) {
                    const int i = 2 * g + e;
                    if (r < S::lo(i) || r >= S::hi(i) || (RS2 && (0x67 >> r & 1) != (h < 3))) continue;
                    asm volatile("v_mfma_f32_16x16x32_bf16 %0, %1, %2, %0" : "+v"(acc[i][r]) : "v"(cat(wl[i], wh[i])), "v"(xf[r]));
                    SB();
                    if (n < 2 && h < 2) { rdw(2 * h + 2 + n, 0); rdw(2 * h + 2 + n, 1); }
                    fi[nf] = i; fw[nf] = r; ++nf;
                    behind(nf - 1);
                    ++n;
                    SB();
                }
        }
        asm volatile("s_nop 7");
#pragma unroll
        for (int f = 0; f < LW; ++f) { behind(nf + f); SB(); }
    };
    TT(9);
    __builtin_amdgcn_s_setprio(LP);
    BARL();
    TT(0); TT(5);
#pragma unroll
    for (int r = R0; r < 9; ++r) {
        if (r - R0 < 4) { rdw((r - R0) >> 1, (r - R0) & 1); SB(); }
        xf[r] = lds_rd128(px0 + r * 2048);
        SB();
    }
#pragma unroll 1
    for (int s = 0; s < 6; ++s) { tick(0, s == 0); tick(1, false); }
    TT(1); TT(6);
    tick(0, false);
    BARK();
    TT(2);
    last();
    TT(3); TT(7);
}

struct AMk { f32x4 c0, c1; u32x4 m; };
__device__ __forceinline__ AMk attn_mask(unsigned ln, bool top) {
    AMk t;
    const int out = (int)(~((0x1feu << (ln & 15)) & (top ? ~0xffu : ~0u)) >> (4 * (ln >> 4)));
    unsigned b0[4], b1[4];
#pragma unroll
    for (int i = 0; i < 4; ++i) {
        b0[i] = (unsigned)__builtin_amdgcn_sbfe(out, i, 1); b1[i] = (unsigned)__builtin_amdgcn_sbfe(out, 16 + i, 1);
        t.c0[i] = __uint_as_float(b0[i] & 0xf149f2cau);
        t.c1[i] = __uint_as_float(b1[i] & 0xf149f2cau);
    }
#pragma unroll
    for (int i = 0; i < 2; ++i) {
        t.m[i] = ~((b0[2 * i] & 0xffffu) | (b0[2 * i + 1] & 0xffff0000u));
        t.m[2 + i] = ~((b1[2 * i] & 0xffffu) | (b1[2 * i + 1] & 0xffff0000u));
    }
    asm volatile("" : "+v"(t.c0), "+v"(t.c1), "+v"(t.m));
    return t;
}
struct AA { u32x4 a; u32x2 b; };
__device__ __forceinline__ AA attn_addr(unsigned ln, int w, int rt, int hg) {
    AA t;
    const unsigned fr = ln & 15, fq = ln >> 4, b = (w >> 1) * 16, z = (w & 1) * 576, gq = ((fq & 1) * 2 + (fq >> 1)) * GP + z, gv = (56 + (ln & 3)) * GP + z;
    t.a[0] = gq + (b + fr) * 8;
    t.a[1] = fq == 3 ? 1152 - 64 : t.a[0];
    t.a[2] = gq + (b + 16 + min(fr, 7u)) * 8;
    t.a[3] = gv + (b + (ln >> 2)) * 8;
    t.b[0] = gv + (b + 16 + min(ln >> 2, 7u)) * 8;
    t.b[1] = (unsigned)(rt * 56 + hg * 14 + fq) * 2048u + (w * 16 + fr) * 16u;
    asm volatile("" : "+v"(t.a), "+v"(t.b));
    return t;
}
#define Q5 tp(0)
#define Q5N 1
#define Q6 tp(1)
#define Q7 tp(2); tp(3)
#define Q8 tw()
#define QD 0
__device__ __forceinline__ unsigned qkv_attn(char* smem, const u16* xA, const u16* wqkv, const char* wo, char* ws, int rt, int hg, int tid, int w,
                                         Hs& hs, int kst, int ro, int co, u64 cb, int qi, WO& wq) {
    const int lane = tid & 63, s0 = rt * 128, c = w & 3;
    auto st = [&](int i) { const int k = kst + i; return k >= 7 ? k - 7 : k; };
    u16* o = (u16*)(ws + WS_O);
    AMk am;
    AA aa;
    unsigned hv2 = 0;
    u64 tt[11] = {0, 0, 0, 0, 0, 0, 0, 0, 0, 0, 0};
    if (w >= 4) {
        if (w == 4) qkv_mma<0>(smem, lane, 0, tt);
        else if (w == 5) qkv_mma<1>(smem, lane, 5, tt);
        else qkv_mma<2>(smem, lane, 7 * w - 33, tt);
        __builtin_amdgcn_s_setprio(0);
        const char* tb = smem + W_A + c * 1024 + lane * 16;
        aa.a = *reinterpret_cast<const u32x4*>(tb); aa.b = *reinterpret_cast<const u32x2*>(tb + 4096);
        tb = smem + Q_TAB + lane * 16;
        am.c0 = *reinterpret_cast<const f32x4*>(tb); am.c1 = *reinterpret_cast<const f32x4*>(tb + 1024);
        am.m = *reinterpret_cast<const u32x4*>(tb + 2048);
        BAR();
        TT(4);
    } else {
#define LP0 1
        __builtin_amdgcn_s_setprio(LP0);
        const char* wg = (const char*)wqkv + ((c >> 1) * 16 + (c & 1) * 4) * 2688 + hg * 224;
        const unsigned offW = (unsigned)(lane * 16 + (lane * 37 >> 9) * (2688 - 224));
        if (QD) __builtin_amdgcn_s_sleep(QD);
        TT(0);
#define XF 1
        if (!XF) { qkv_reqW(smem, wg, offW, c, 2 * kst, 1); SB(); }
        const QX qx = qkv_offX(c, lane, s0);
        qkv_reqX(smem, xA, qx, c, kst, 0);
        if (XF) { SB(); qkv_reqW(smem, wg, offW, c, 2 * kst, 1); }
        TT(1);
        qkv_reqW(smem, wg, offW, c, 2 * kst + 1, 2);
        TT(2);
        wait_vm<6>();
        TT(4);
        BARL();
        __builtin_amdgcn_s_setprio(0);
        STAMP(0, 1);
        unsigned hv = 0;
        if (w == 0) { hs.off = hs_off(lane, HS_O + rt * 256, 0, ro, co); hs_rmw(hs, hv, M_R14 | M_OW); }
#define XO 0
        const bool late = XO == 1 || (XO == 2 && ((hg + 1) & 2));
        if (!late) qkv_reqX(smem, xA, qx, c, st(1), 1);
        qkv_reqW(smem, wg, offW, c, 2 * st(1), 3);
        if (late) wait_vm<6>(); else wait_vm<10>();
        BARL();
        STAMP(0, 9);
        int sa = 4, sx = late ? 1 : 2;
        const unsigned tc = (unsigned)((c * 32 + (lane >> 1)) * 4096), ts = (lane & 1) * 4;
        const unsigned tb = (TQ + qi * 0x11111111u) & 0x77777777u | 0x88888888u;
        auto tp = [&](int i) { asm volatile("s_mov_b32 m0, %2\n\ts_nop 0\n\tglobal_load_lds_dword %0, %1" ::"v"(tc + ((tb >> 8 * i >> ts & 15) << 7)), "s"(cb), "i"(Q_DUMMY) : "memory"); };
#pragma unroll 1
        for (int s = 0; s < 5; ++s) {
            if (late) qkv_reqX(smem, xA, qx, c, st(s + 1), sx);
            qkv_reqW(smem, wg, offW, c, 2 * st(s + 1) + 1, sa);
            qkv_reqW(smem, wg, offW, c, 2 * st(s + 2), sa == 4 ? 0 : sa + 1);
            if (late) wait_vm<6>();
            else {
                qkv_reqX(smem, xA, qx, c, st(s + 2), sx);
                if (c < 2) wait_vm<11>(); else wait_vm<10>();
            }
            sa = sa >= 3 ? sa - 3 : sa + 2; sx = sx == 2 ? 0 : sx + 1;
            if (w == 0 && s == 3) { hs.snap = hs_take<63>(hv); hs_add(hs, M_OR); }
            BARL();
        }
        if (late) qkv_reqX(smem, xA, qx, c, st(6), 0);
        qkv_reqW(smem, wg, offW, c, 2 * st(6) + 1, 4);
        int l2 = lane;
        Q5;
        asm volatile("" : "+v"(l2));
        __builtin_assume(l2 >= 0 && l2 < 64);
        am = attn_mask(l2, rt + (w >> 1) == 0);
        aa = attn_addr(l2, w, rt, hg);
        const AA aa2 = attn_addr(l2, w + 4, rt, hg);
        unsigned wt = ((qi * 8 + w) * 49 + min(l2, 48)) * 128;
        asm volatile("" : "+v"(wt));
        auto tw = [&]() {
            asm volatile("s_mov_b32 m0, %2\n\ts_nop 0\n\tglobal_load_lds_dword %0, %1" ::"v"(wt), "s"(wo), "i"(Q_DUMMY) : "memory");
            asm volatile("s_mov_b32 m0, %2\n\ts_nop 0\n\tglobal_load_lds_dword %0, %1" ::"v"(wt), "s"(wo + 4 * 49 * 128), "i"(Q_DUMMY) : "memory");
        };
        wait_vm<Q5N>();
        BARL();
        Q6;
        {
            char* tq = smem + W_A + c * 1024 + l2 * 16;
            *reinterpret_cast<u32x4*>(tq) = aa2.a; *reinterpret_cast<u32x2*>(tq + 4096) = aa2.b;
            if (w == 3) {
                tq = smem + Q_TAB + l2 * 16;
                *reinterpret_cast<f32x4*>(tq) = am.c0; *reinterpret_cast<f32x4*>(tq + 1024) = am.c1; *reinterpret_cast<u32x4*>(tq + 2048) = am.m;
            }
        }
        BAR();
        if (w == 3) *reinterpret_cast<u32x2*>(smem + ((l2 & 3) * 7 + 4) * GP + 1152) = u32x2{0u, 0u};
        Q7;
        if (w == 0) hs_rmw(hs, hv2, M_R14);
        TT(3);
        BAR();
        Q8;
    }
    const unsigned m0[2] = {am.m[0], am.m[1]}, m1[2] = {am.m[2], am.m[3]};
    const f32x4 c0 = am.c0, c1 = am.c1;
    const float scale = 0.18898223650461361f, log2e = 1.4426950408889634f;
#define FTA(i)
    FTA(0);
    auto rd64 = [&](unsigned a, int g) { return *reinterpret_cast<const s16x4*>(smem + a + g * GP); };
    bf16x8 qf[4], k0[4], k1[4], v0[4], v1[4];
#pragma unroll
    for (int hh = 0; hh < 4; ++hh) {
        qf[hh] = cat(rd64(aa.a[0] + 64, 7 * hh), rd64(aa.a[1] + 64, 7 * hh + 4));
        k0[hh] = cat(rd64(aa.a[0], 28 + 7 * hh), rd64(aa.a[0], 32 + 7 * hh));
        k1[hh] = cat(rd64(aa.a[2], 28 + 7 * hh), rd64(aa.a[2], 32 + 7 * hh));
        SB();
    }
#pragma unroll
    for (int hh = 0; hh < 4; ++hh) {
        v0[hh] = cat(lds_tr(smem + aa.a[3] + 7 * hh * GP), lds_tr(smem + aa.b[0] + 7 * hh * GP));
        v1[hh] = cat(lds_tr(smem + aa.a[3] + (7 * hh + 4) * GP), lds_tr(smem + aa.b[0] + (7 * hh + 4) * GP));
    }
    SB();
    FTA(1);
#define AB2 1
    if (RS2 && AB2 == 0 && w < 4) { SB(); BARL(); SB(); }
    float l[2][4], mx[2];
#pragma unroll
    for (int hp = 0; hp < 2; ++hp) {
        const f32x4 a0 = MFMA(k0[2 * hp], qf[2 * hp], c0), a1 = MFMA(k1[2 * hp], qf[2 * hp], c1);
        const f32x4 b0 = MFMA(k0[2 * hp + 1], qf[2 * hp + 1], c0), b1 = MFMA(k1[2 * hp + 1], qf[2 * hp + 1], c1);
        float t[4];
#pragma unroll
        for (int i = 0; i < 4; ++i) {
            auto r = __builtin_amdgcn_permlane32_swap(__float_as_uint(vmax(a0[i], a1[i])), __float_as_uint(vmax(b0[i], b1[i])), false, false);
            t[i] = vmax(__uint_as_float(r[0]), __uint_as_float(r[1]));
        }
#pragma unroll
        for (int i = 0; i < 2; ++i) {
            unsigned a = pk2(t[2 * i], t[2 * i + 1]);
            a = pk2(__uint_as_float(a << 16) * scale, __uint_as_float(a & 0xffff0000u) * scale);
            l[hp][2 * i] = __uint_as_float(a << 16);
            l[hp][2 * i + 1] = __uint_as_float(a & 0xffff0000u);
        }
        mx[hp] = vmax(vmax3(l[hp][0], l[hp][1], l[hp][2]), l[hp][3]);
    }
    {
        auto r = __builtin_amdgcn_permlane16_swap(__float_as_uint(mx[0]), __float_as_uint(mx[1]), false, false);
        const float c = vmax(__uint_as_float(r[0]), __uint_as_float(r[1])) * -log2e;
        auto q2 = __builtin_amdgcn_permlane16_swap(__float_as_uint(c), __float_as_uint(c), false, false);
        mx[0] = __uint_as_float(q2[0]); mx[1] = __uint_as_float(q2[1]);
    }
    if (RS2 && AB2 == 1 && w < 4) { SB(); BARL(); SB(); }
    float sm[2];
#pragma unroll
    for (int hp = 0; hp < 2; ++hp) {
#pragma unroll
        for (int i = 0; i < 4; ++i) l[hp][i] = __builtin_amdgcn_exp2f(__builtin_fmaf(l[hp][i], log2e, mx[hp]));
        sm[hp] = (l[hp][0] + l[hp][1]) + (l[hp][2] + l[hp][3]);
    }
    {
        auto r = __builtin_amdgcn_permlane16_swap(__float_as_uint(sm[0]), __float_as_uint(sm[1]), false, false);
        const float c = __builtin_amdgcn_rcpf(__uint_as_float(r[0]) + __uint_as_float(r[1]));
        auto q2 = __builtin_amdgcn_permlane16_swap(__float_as_uint(c), __float_as_uint(c), false, false);
        sm[0] = __uint_as_float(q2[0]); sm[1] = __uint_as_float(q2[1]);
    }
    FTA(2);
    const f32x4 z = {0.f, 0.f, 0.f, 0.f};
    const __amdgpu_buffer_rsrc_t rs_o = __builtin_amdgcn_make_buffer_rsrc(o, 0, 2048 * 448 * 2, 0x00020000);
    const bool top = aa.a[0] != aa.a[1];
#pragma unroll
    for (int hp = 0; hp < 2; ++hp) {
        const unsigned p01 = pk2(l[hp][0] * sm[hp], l[hp][1] * sm[hp]), p23 = pk2(l[hp][2] * sm[hp], l[hp][3] * sm[hp]);
        auto e01 = __builtin_amdgcn_permlane32_swap(p01, p01, false, false);
        auto e23 = __builtin_amdgcn_permlane32_swap(p23, p23, false, false);
        u32x4 ov[2];
#pragma unroll
        for (int e = 0; e < 2; ++e) {
            const int hh = 2 * hp + e;
            u32x4 pw = {e01[e] & m0[0], e23[e] & m0[1], e01[e] & m1[0], e23[e] & m1[1]};
            const bf16x8 pf = __builtin_bit_cast(bf16x8, pw);
            const f32x4 o0 = MFMA(v0[hh], pf, z), o1 = MFMA(v1[hh], pf, z);
            ov[e] = u32x4{pk2(o0[0], o0[1]), pk2(o0[2], o0[3]), pk2(o1[0], o1[1]), pk2(o1[2], o1[3])};
        }
        ov[0][2] = top ? ov[1][0] : ov[0][2]; ov[0][3] = top ? ov[1][1] : ov[0][3];
        __builtin_amdgcn_raw_buffer_store_b128(ov[0], rs_o, aa.b[1], hp * 7 * 2048, 16);
        if (!top) __builtin_amdgcn_raw_buffer_store_b128(ov[1], rs_o, aa.b[1], (hp * 7 + 4) * 2048, 16);
    }
    FTA(4);
    if (w >= 4) {
        int l2 = lane;
        asm volatile("" : "+v"(l2));
        __builtin_assume(l2 >= 0 && l2 < 64);
        wo_init(wq, l2, c);
#pragma unroll 1
        for (int t = 0; t < WE; ++t) wo_req(wq, smem + W_A + t * 4096 + c * 1024);
    } else wait_vm<0>();
    const unsigned hv3 = hs_take<WE>(hv2);
    FTA(5);
    BARL();
    return hv3;
}
__device__ __forceinline__ void oproj(char* smem, u16* __restrict__ o, u16* __restrict__ out, int rt, int rg, int ct, int lane) {
    const int fr = lane & 15, fq = lane >> 4;
    const __amdgpu_buffer_rsrc_t rs_o = __builtin_amdgcn_make_buffer_rsrc(o, 0, 2048 * 448 * 2, 0x00020000);
    const unsigned ga = (unsigned)(rt * 56 + fq) * 2048u + (unsigned)(rg * 16 + fr) * 16u;
    bf16x8 fa[14];
#pragma unroll
    for (int ks = 0; ks < 14; ++ks) fa[ks] = __builtin_bit_cast(bf16x8, __builtin_amdgcn_raw_buffer_load_b128(rs_o, ga + ks % 7 * 8192u, ks / 7 * 57344, 16));
    SB();
    const int q = (lane >> 2) & 3, p = lane & 3, s = 2 * (fq & 1) + (q >> 1);
    const char* aw[4];
#pragma unroll
    for (int c = 0; c < 4; ++c) aw[c] = smem + (8 * fq + q) * 128 + ((c ^ s) * 32) + 8 * p;
    f32x4 acc[4];
#pragma unroll
    for (int c = 0; c < 4; ++c) acc[c] = f32x4{0.f, 0.f, 0.f, 0.f};
    constexpr int OA = 9;
    bf16x8 fb[14][4];
#pragma unroll
    for (int ks = 0; ks < 14 + OA; ++ks) {
        if (ks >= OA) {
#pragma unroll
            for (int c = 0; c < 4; ++c) acc[c] = MFMA(fb[ks - OA][c], fa[ks - OA], acc[c]);
        }
        if (ks < 14) {
#pragma unroll
            for (int c = 0; c < 4; ++c) fb[ks][c] = cat(lds_tr(aw[c] + ks * 4096), lds_tr(aw[c] + ks * 4096 + 512));
        }
        SB();
    }
    STAMP(1, 3);
    const __amdgpu_buffer_rsrc_t rs_out = __builtin_amdgcn_make_buffer_rsrc(out, 0, 2048 * 896 * 2, 0x00020000);
    const unsigned ob = (unsigned)((rt * 128 + (rg >> 1) * 32 + 2 * (fr & 7) + (rg & 1)) * 896 + ct * 64 + (fr & 8) * 4 + (fq & 1) * 16 + (fq >> 1) * 8) * 2u;
    u32x4 v[2];
#pragma unroll
    for (int pr = 0; pr < 2; ++pr) {
        const f32x4 a = acc[2 * pr], b = acc[2 * pr + 1];
        auto x = __builtin_amdgcn_permlane16_swap(pk2(a[0], a[1]), pk2(b[0], b[1]), false, false);
        auto y = __builtin_amdgcn_permlane16_swap(pk2(a[2], a[3]), pk2(b[2], b[3]), false, false);
        v[pr] = u32x4{x[0], y[0], x[1], y[1]};
    }
    u32x4 p0, p1;
#pragma unroll
    for (int i = 0; i < 4; ++i) {
        p0[i] = __builtin_amdgcn_update_dpp(v[0][i], v[1][i], 0x128, 0xf, 0xc, false);
        p1[i] = __builtin_amdgcn_update_dpp(v[1][i], v[0][i], 0x128, 0xf, 0x3, false);
    }
    st16_out<16>(rs_out, ob, p0);
    st16_out<16>(rs_out, ob + 16 * 1792u, p1);
}

__global__ __launch_bounds__(512, 2) void kf(const u16* xA, const u16* wqkv, const char* xB, const char* Hr, const char* Hi, const char* Cr,
                                          const char* Ci, const char* wo, u16* out, char* ws) {
    __shared__ __attribute__((aligned(16))) char smem[LDS_TOTAL];
    int tid = threadIdx.x, bid = blockIdx.x;
    u64 pc0;
    {
        unsigned part, off;
        asm volatile("s_getpc_b64 %0\n\ts_bfe_u32 %1, %2, 0x30003\n\ts_mulk_i32 %1, %3" : "=&s"(pc0), "=&s"(part), "+s"(bid) : "i"(CODE_PART) : "scc");
        asm volatile("s_mov_b32 m0, %4\n\tv_mad_u32_u24 %0, %1, 12, %2\n\tglobal_load_lds_dword %0, %3" : "=&v"(off), "+v"(tid) : "s"(part), "s"(pc0), "i"(Q_DUMMY) : "memory");
        asm volatile("" : "+s"(bid));
    }
    __builtin_assume(tid >= 0 && tid < 512 && bid >= 0 && bid < 256);
    const int lane = tid & 63, xcd = bid & 7;
    int w = __builtin_amdgcn_readfirstlane(tid >> 6), j = bid >> 3;
    STAMP(0, 0);
    const int ks = xcd & 3, rl = j & 3, c6 = j >> 2, r8 = (xcd >> 2) * 4 + rl;
    BL q;
    if (j < 24 && w < 4) {
        unsigned ox = (unsigned)(((ks & 1) * 1024 + r8 * 64) * 896), oh = (unsigned)((c6 < 5 ? c6 * 80 : 384) * 896);
#define KR 1
        q.kc = c6 + KR * rl >= 7 ? c6 + KR * rl - 7 : c6 + KR * rl;
        b_off(q, c6 < 4 ? 79 : 63, lane, w);
        const char *p0 = xB, *p1 = Hr, *p2 = Hi;
        asm volatile("" : "+v"(q.offX), "+v"(q.offH[0]), "+v"(q.offH[1]), "+v"(q.offH[2]));
        asm volatile("" : "+s"(p0), "+s"(p1), "+s"(p2), "+s"(ox), "+s"(oh), "+s"(q.kc));
        q.xg = p0 + ox;
        q.hg = ((ks >> 1) ? p2 : p1) + oh;
        STAMP(0, 7);
        reqH(q, smem, w, 0); reqX(q, smem, w, 0);
        reqH(q, smem, w, 1); reqX(q, smem, w, 1);
        asm volatile("" ::"s"(xA), "s"(wqkv), "s"(Cr), "s"(Ci), "s"(wo), "s"(out), "s"(ws));
    }
    asm volatile("" : "+s"(w), "+s"(j));
    __builtin_assume(w >= 0 && w < 8 && j >= 0 && j < 32);
    if (w == 7) *reinterpret_cast<u32x2*>(smem + L_GO) = u32x2{0u, 0u};
    Hs hs;
    hs.base = (const unsigned*)(ws + WS_HS); hs.off = 0; hs.snap = 0;
    int ro = 0, rg = 0, ct = 0;
    bool duty = true;
    u64 again = 0, mr = M_R14;
    unsigned ka = 0, kb = 0;
    int wb = W_0;
    if (j < 24) {
        u16* ab = (u16*)ws;
        u16* px = (u16*)(ws + WS_PX);
        const int rbl = j & 7, cg = j >> 3, rb = (xcd >> 2) * 8 + rbl;
#define DU 1
        duty = DU ? (j & 19) != 3 : j >= 4;
        const int un = DU ? j - min(c6, 4) : j - 4, uc = un >= 14 ? un - 14 : un;
        ro = xcd * 2 + (un >= 14); ct = (uc >> 1) & 7; rg = (uc & 1) * 4 + (w & 3);
        const int g1 = ks * 3 + cg;
        if (w < 4) {
            const char* cgp = ((ks >> 1) ? Ci : Cr) + (long)rb * (128 * 4096) + (ks & 1) * 2048;
            b_load(smem, q, cgp, (const char*)ab + g1 * (16 * B_SL), (u64)(((cg == 0 ? TD0 : cg == 1 ? TD1 : TD2) + rbl * 0x11111111u) & 0x77777777u) << 32 |
                   (((cg == 0 ? TC0 : cg == 1 ? TC1 : TC2) + rbl * 0x11111111u) & 0x77777777u), ks, cg == 2,
                   wo + (duty ? ct : 0) * 128, duty, hs, lane, w, r8 * 2 + (c6 & 1));
        } else {
            const int cw = w - 4;
            asm volatile("" ::"s"(xB), "s"(Hr), "s"(Hi));
            hs.off = hs_off(lane, g1 * 256, HS_X + ((rb * 3 + cg) * 4 + cw) * 256, ro, uc & 15);
            unsigned v1 = 0;
            hs_rmw(hs, v1, w == 5 ? M_LO | M_HI | M_XW | M_XR | M_OW : M_XW | M_XR);
            STAMP(0, 7);
            auto post = [&]() {
                if (w == 5) *reinterpret_cast<u32x2*>(smem + L_KEY + lane * 8) = u32x2{hs.off, hs_take<0>(v1)};
            };
            const unsigned dbase = (unsigned)((g1 * 16 + r8 * 2) * B_SL + (c6 & 1) * (c6 < 4 ? 80 : 64) * 64);
            const unsigned sg = (ks >> 1) ? 0x80008000u : 0u;
            if (c6 < 4 && !(cw & 1)) ab_mma<3>(smem, dbase, sg, lane, cw >> 1, 0, ab, post);
            else ab_mma<2>(smem, dbase, sg, lane, cw >> 1, (cw & 1) * (c6 < 4 ? 3 : 2), ab, post);
            hs.snap = hs_take<63>(v1);
            hs_add(hs, 1ull << (36 + ks));
            const unsigned pxb = (unsigned)(rb * 3 + cg) * 131072u, ob0 = (unsigned)((rb * 128 + ks * 16) * 896 + 448 + cg * 160) * 2u;
            if (cg == 2) y_mma<4>(smem, lane, cw, px, pxb, ks, out, ob0, hs);
            else y_mma<5>(smem, lane, cw, px, pxb, ks, out, ob0, hs);
            duty = false;
        }
    } else {
        const int qi = j - 24;
#define HGM 0x3102
        const int hg = HGM >> (4 * ks) & 3, rt = (xcd >> 2) * 8 + qi;
#define XT4 0x5401
        int kst = qi + (XT4 >> (4 * hg) & 15); kst = kst >= 7 ? kst - 7 : kst;
        ro = xcd * 2 + 1; ct = 3 + (qi >> 1); rg = (qi & 1) * 4 + (w & 3); duty = w < 4;
        const int co = 6 + qi;
        WO wq;
        wq.g = wo + ct * 128;
        const unsigned hv = qkv_attn(smem, xA, wqkv, wo, ws, rt, hg, tid, w, hs, kst, ro, co,
                                     (u64)(((ks >> 1) ? Ci : Cr) + (long)((xcd >> 2) * 8 + qi) * (128 * 4096) + (ks & 1) * 2048), qi, wq);
        STAMP(0, 21);
        if (w == 0) {
            hs_rmw(hs, ka, 1ull << hg | M_OW, lane == hg);
            if (!hs_new(hs, hv, mr)) again = 1ull << hg;
        }
        if (w >= 4) {
#pragma unroll 1
            for (int t = WE; t < 14; ++t) wo_req(wq, smem + W_A + t * 4096 + (w & 3) * 1024);
            wait_vm<0>();
        }
        BARL();
        wb = W_A;
    }
    if (duty) {
        volatile LDS_AS unsigned* go = (volatile LDS_AS unsigned*)(smem + L_GO);
        if (w == 0) { if (j < 24) { hs_rmw(hs, ka, M_OW); __builtin_amdgcn_s_sleep(PD2); } hs_wait2(hs, M_OW, M_OW, 0u, ka, kb); *go = 1u; }
        else lds_wait<SL2>(L_GO);
        asm volatile("" ::: "memory");
        STAMP(1, 0);
        if (w == 0) hs_add(hs, M_OR);
        oproj(smem + wb, (u16*)(ws + WS_O), out, ro, rg, ct, lane);
        hs_keep(ka, kb);
        if (again) { hs_wait(hs, mr, mr, 0u); hs_add(hs, again); }
    }
    STAMP(1, 24);
}

extern "C" void kernel_launch(void* const* d_in, const int* in_sizes, int n_in, void* d_out, int out_size,
                              void* d_ws, size_t ws_size, hipStream_t stream) {
    kf<<<256, 512, 0, stream>>>((const u16*)d_in[0], (const u16*)d_in[1], (const char*)d_in[3], (const char*)d_in[6], (const char*)d_in[7],
                                (const char*)d_in[4], (const char*)d_in[5], (const char*)d_in[2], (u16*)d_out, (char*)d_ws);
}
```

```cpp
#include <hip/hip_runtime.h>
#include <hip/hip_bf16.h>

typedef unsigned short u16;
typedef __attribute__((ext_vector_type(8))) short bf16x8;
typedef __attribute__((ext_vector_type(4))) short s16x4;
typedef __attribute__((ext_vector_type(4))) float f32x4;
typedef __attribute__((ext_vector_type(2))) __bf16 bf2;
typedef __attribute__((ext_vector_type(2))) unsigned u32x2;
typedef __attribute__((ext_vector_type(4))) unsigned u32x4;

#define LDS_AS __attribute__((address_space(3)))
#define GLB_AS __attribute__((address_space(1)))

constexpr int D = 448;

#define STAMP(kern, idx)
#define STAMPC(kern, idx)

typedef __attribute__((ext_vector_type(2))) float f32x2;
__device__ __forceinline__ unsigned pk2(float a, float b) {
    f32x2 v = {a, b};
    return __builtin_bit_cast(unsigned, __builtin_convertvector(v, bf2));
}
__device__ __forceinline__ void glds16a(const void* sbase, unsigned voff, const char* l) {
    const unsigned la = (unsigned)(size_t)(LDS_AS const char*)l;
    asm volatile("s_mov_b32 m0, %2\n\ts_nop 0\n\tglobal_load_lds_dwordx4 %0, %1" ::"v"(voff), "s"(sbase), "s"(la) : "memory");
}
__device__ __forceinline__ float vmax(float a, float b) { return __builtin_elementwise_maximum(a, b); }
__device__ __forceinline__ float vmax3(float a, float b, float c) { return vmax(vmax(a, b), c); }
__device__ __forceinline__ bf16x8 lds_rd128(const char* p) { return *reinterpret_cast<const bf16x8*>(p); }
__device__ __forceinline__ s16x4 lds_tr(const char* p) {
    return __builtin_amdgcn_ds_read_tr16_b64_v4i16((LDS_AS s16x4*)p);
}
__device__ __forceinline__ bf16x8 cat(s16x4 a, s16x4 b) {
    bf16x8 r;
    r[0] = a[0]; r[1] = a[1]; r[2] = a[2]; r[3] = a[3];
    r[4] = b[0]; r[5] = b[1]; r[6] = b[2]; r[7] = b[3];
    return r;
}
#define MFMA(a, b, c) __builtin_amdgcn_mfma_f32_16x16x32_bf16((a), (b), (c), 0, 0, 0)
template <int N> __device__ __forceinline__ void wait_vm() { asm volatile("s_waitcnt vmcnt(%0)" ::"i"(N) : "memory"); }
#define SB() __builtin_amdgcn_sched_barrier(0)
template <int NP, int NM> __device__ __forceinline__ void ilv() {
#pragma unroll
    for (int i = 0; i < NP; ++i) {
        __builtin_amdgcn_sched_group_barrier(0x8, 1, 0);
        __builtin_amdgcn_sched_group_barrier(0x100, 1, 0);
    }
    if constexpr (NM > 0) __builtin_amdgcn_sched_group_barrier(0x8, NM, 0);
}
#define BAR()                                               \
    do {                                                    \
        asm volatile("s_waitcnt lgkmcnt(0)" ::: "memory");  \
        __builtin_amdgcn_s_barrier();                       \
        asm volatile("" ::: "memory");                      \
    } while (0)
#define BARL()                                              \
    do {                                                    \
        asm volatile("" ::: "memory");                      \
        __builtin_amdgcn_s_barrier();                       \
        asm volatile("" ::: "memory");                      \
    } while (0)

#define BARK() do { __builtin_amdgcn_s_waitcnt(0xc07f); asm volatile("" ::: "memory"); __builtin_amdgcn_s_barrier(); asm volatile("" ::: "memory"); SB(); } while (0)
__device__ __forceinline__ int kperm(int r) { return (r & ~12) | ((r & 4) << 1) | ((r & 8) >> 1); }

constexpr int LDS_TOTAL = 163840;
constexpr int WS_O = 4 << 20, WS_PX = 8 << 20, WS_HS = 16 << 20;
#define CODEMAX 41984u
constexpr unsigned CODE_MAX = CODEMAX;

typedef unsigned long long u64;
struct Hs {
    const unsigned* base;
    unsigned off, snap;
};
constexpr int HS_O = 16 * 256, HS_X = 32 * 256;
constexpr u64 M_LO = 0xffffull, M_HI = 0xffff0000ull, M_OW = 0xfull << 48, M_OR = 1ull << 52, M_R14 = 0x3fffull << 16;
constexpr u64 M_XW = 0xfull << 32, M_XR = 0xf0ull << 32;
__device__ __forceinline__ unsigned hs_off(int lane, int b, int bx, int ro, int co) {
    const int bo = HS_O + ro * 256 - 48 * 4;
    return (unsigned)(lane * 4 + (lane < 32 ? b : lane < 48 ? bx - 32 * 4 : lane == 52 ? bo + 48 + co * 4 : bo));
}
template <int N> __device__ __forceinline__ unsigned hs_take(unsigned v) {
    unsigned r;
    asm volatile("s_waitcnt vmcnt(%2)\n\tv_mov_b32 %0, %1" : "=v"(r) : "v"(v), "i"(N) : "memory");
    return r;
}
__device__ __forceinline__ void hs_rmw(const Hs& h, unsigned& v, u64 m, unsigned d = 0u) {
    asm volatile("s_mov_b64 exec, %4\n\tglobal_atomic_add %0, %1, %2, %3 sc0 sc1\n\ts_mov_b64 exec, -1" : "+v"(v) : "v"(h.off), "v"(d), "s"(h.base), "s"(m) : "memory");
}
__device__ __forceinline__ void hs_add(const Hs& h, u64 m) {
    const unsigned one = 1u;
    asm volatile("s_mov_b64 exec, %3\n\tglobal_atomic_add %0, %1, %2 sc1\n\ts_mov_b64 exec, -1" ::"v"(h.off), "v"(one), "s"(h.base), "s"(m) : "memory");
}
__device__ __forceinline__ bool hs_new(const Hs& h, unsigned v, u64 m) { return (__ballot(v == h.snap) & m) == 0; }
#define PSL 2
__device__ __forceinline__ void hs_wait(const Hs& h, u64 m, u64 e, unsigned d) {
#pragma unroll 1
    for (unsigned spin = 0; spin < 40000u; ++spin) {
        unsigned v = 0;
        hs_rmw(h, v, e, d);
        if (hs_new(h, hs_take<0>(v), m)) return;
        __builtin_amdgcn_s_sleep(PSL);
    }
}
#define PD2 5
__device__ __forceinline__ void hs_wait2(const Hs& h, u64 m, u64 e, unsigned d, unsigned& a, unsigned& b) {
    unsigned n = 40000u;
    asm volatile("s_mov_b64 exec, %8\n"
                 "1:\n\tglobal_atomic_add %1, %3, %4, %5 sc0 sc1\n\ts_waitcnt vmcnt(1)\n\tv_cmp_eq_u32 vcc, %0, %6\n\ts_and_b64 vcc, vcc, %7\n\ts_cbranch_scc0 2f\n\t"
                 "global_atomic_add %0, %3, %4, %5 sc0 sc1\n\ts_waitcnt vmcnt(1)\n\tv_cmp_eq_u32 vcc, %1, %6\n\ts_and_b64 vcc, vcc, %7\n\ts_cbranch_scc0 2f\n\t"
                 "s_sub_u32 %2, %2, 1\n\ts_cbranch_scc0 1b\n"
                 "2:\n\ts_mov_b64 exec, -1"
                 : "+&v"(a), "+&v"(b), "+&s"(n) : "v"(h.off), "v"(d), "s"(h.base), "v"(h.snap), "s"(m), "s"(e) : "memory", "vcc", "scc");
}
__device__ __forceinline__ void hs_keep(unsigned a, unsigned b) { asm volatile("s_waitcnt vmcnt(0)" ::"v"(a), "v"(b) : "memory"); }
template <int POL> __device__ __forceinline__ void st16_out(__amdgpu_buffer_rsrc_t r, unsigned off, u32x4 v) {
    __builtin_amdgcn_raw_buffer_store_b128(v, r, off, 0, POL);
}
__device__ __forceinline__ void st16_sc1(__amdgpu_buffer_rsrc_t r, unsigned off, u32x4 v) {
    __builtin_amdgcn_raw_buffer_store_b128(v, r, off, 0, 16);
}
__device__ __forceinline__ void glds16m(const void* sbase, unsigned voff, const char* l, u64 mask) {
    const unsigned la = (unsigned)(size_t)(LDS_AS const char*)l;
    asm volatile("s_mov_b32 m0, %2\n\ts_mov_b64 exec, %3\n\tglobal_load_lds_dwordx4 %0, %1\n\ts_mov_b64 exec, -1" ::"v"(voff), "s"(sbase), "s"(la), "s"(mask) : "memory");
}
__device__ __forceinline__ void glds16c(const void* sbase, unsigned voff, const char* l) {
    const unsigned la = (unsigned)(size_t)(LDS_AS const char*)l;
    asm volatile("s_mov_b32 m0, %2\n\ts_nop 0\n\tglobal_load_lds_dwordx4 %0, %1 sc1" ::"v"(voff), "s"(sbase), "s"(la) : "memory");
}
__device__ __forceinline__ void glds16mc(const void* sbase, unsigned voff, const char* l, u64 mask) {
    const unsigned la = (unsigned)(size_t)(LDS_AS const char*)l;
    asm volatile("s_mov_b32 m0, %2\n\ts_mov_b64 exec, %3\n\tglobal_load_lds_dwordx4 %0, %1 sc1\n\ts_mov_b64 exec, -1" ::"v"(voff), "s"(sbase), "s"(la), "s"(mask) : "memory");
}
template <int SL> __device__ __forceinline__ void lds_wait(unsigned a) {
    unsigned n = 1000000u, v;
    asm volatile("1:\n\ts_sleep %3\n\tds_read_b32 %0, %2\n\ts_sub_u32 %1, %1, 1\n\ts_cbranch_scc1 2f\n\ts_waitcnt lgkmcnt(0)\n\tv_cmp_eq_u32 vcc, 0, %0\n\ts_cbranch_vccnz 1b\n"
                 "2:\n\ts_waitcnt lgkmcnt(0)" : "=&v"(v), "+s"(n) : "v"(a), "i"(SL) : "memory", "vcc", "scc");
}

constexpr int A_SL = 16384, B_R0 = 6 * A_SL, B_SL = 20480, X_SL = 8192, H_SL = 10240, H_0 = 11 * X_SL, L_KEY = H_0 + 7 * H_SL, L_GO = 163328, W_0 = A_SL;
constexpr int CODE_PART = (CODE_MAX - 6144) / 7 / 128 * 128;
#define TT(i)
struct WO { const char* g; u64 pat; unsigned off, hi; };
__device__ __forceinline__ void wo_init(WO& q, int lane, int w) {
    q.g += w * (8 * 896);
    q.pat = 0ull;
#pragma unroll
    for (int i = 0; i < 21; ++i) q.pat |= (u64)(i % 7 == 6 ? 7 : (i % 7 + 1) / 2) << (3 * i);
    q.pat >>= 6 * w;
    q.hi = (unsigned)(lane >> 5) * (3 * 896);
    q.off = (unsigned)(((lane >> 5) * 16 + ((lane >> 3) & 3)) * 896 + ((lane & 7) ^ ((w & 1) * 4 + ((lane >> 4) & 1) * 2)) * 16);
    asm volatile("" : "+v"(q.off), "+v"(q.hi));
}
__device__ __forceinline__ void wo_req(WO& q, const char* l) {
    glds16a(q.g - ((unsigned)q.pat & 3) * (4 * 896), q.off + ((unsigned)q.pat & 4) * q.hi, l);
    q.g += 32 * 896; q.pat >>= 3;
}
#define SL1 1
#define SL2 1
#define N4 3
#define N5 4
#define N6 5
#define KA 3
#define V1 1
#define PDG 1
#define DEF 2
#define TC0 0x75643210u
#define TC1 0x67540132u
#define TD0 0x76543210u
#define TD1 TD0
#define TD2 TD0
#define TQ 0x76543210u
#define TC2 0x54763201u
#define LP 2
#define WP1 3
#define WP2 10
#define WE 2
struct BL {
    const char *xg, *hg;
    unsigned offX, offH;
    int kc, h4;
};
__device__ __forceinline__ void reqX(BL& q, char* smem, int w, int sx) {
    const char* gx = q.xg + q.kc * 128;
    const char* la = smem + sx * X_SL + w * 2048;
    glds16a(gx, q.offX, la);
    glds16a(gx + 8 * 896, q.offX, la + 1024);
    q.kc = q.kc == 6 ? 0 : q.kc + 1;
}
__device__ __forceinline__ void reqH(BL& q, char* smem, int w, int k) {
    const char* gh = q.hg + q.kc * 128;
    const char* lh = smem + H_0 + k * H_SL + w * 2048;
    glds16a(gh, q.offX, lh);
    glds16a(gh + 8 * 896, q.offX, lh + 1024);
    glds16m(gh + q.h4, q.offH, lh + 8192 - w * 1536, 0xffffffffull);
}
__device__ __forceinline__ void b_off(BL& q, int lane, int w) {
    const int r8 = lane >> 3, c = (lane & 7) ^ r8;
    q.offX = (unsigned)((w * 16 + r8) * 896 + c * 16);
    q.offH = (unsigned)((w * 4 + (r8 & 3)) * 896 + (c ^ ((w & 1) * 4)) * 16);
}
__device__ __forceinline__ void b_load(char* smem, BL& q, const char* cg, const char* abg, u64 tb, int ks,
                                       bool narrow, const char* wog, bool duty, Hs& hs, int lane, int w, int pm) {
    const int r8 = lane >> 3, c = (lane & 7) ^ r8;
    unsigned offA[4];
#pragma unroll
    for (int i = 0; i < 4; ++i)
        offA[i] = (unsigned)(((w >> 1) * 64 + (((w & 1) * 2 + (i >> 1) + ks) & 3) * 16 + (i & 1) * 8 + r8) * 4096 + c * 16);
    const int rb0 = w * (narrow ? 32 : 40);
    unsigned offB = (unsigned)((c >> 2) * 10240 + (rb0 + r8) * 64 + (c & 3) * 16);
    u64 mB = narrow ? 1ull : ~0ull;
    int sa = 3;
    u64 tc = tb | 0x8888888800000000ull;
    const unsigned t3 = (unsigned)tb;
    auto reqC = [&]() {
        const char* g = cg + ((unsigned)tc & 15) * 128;
        const char* la = smem + sa * A_SL + w * 4096;
#pragma unroll
        for (int i = 0; i < 4; ++i) glds16a(g, offA[i], la + i * 1024);
        sa = sa == 5 ? 0 : sa + 1; tc >>= 4;
    };
    int sb = 0, lb0 = B_R0 + rb0 * 128;
    tb = tb << 1 | 0x1111111100000000ull;
    auto reqB = [&]() {
        const char* g = abg + ((unsigned)tb & 15) * B_SL;
        const char* lb = smem + lb0 + sb * B_SL;
#pragma unroll
        for (int i = 0; i < 4; ++i) glds16c(g + i * 512, offB, lb + i * 1024);
        glds16mc(g + 2048, offB, lb + 4096, mB);
        sb = sb == 2 ? 0 : sb + 1; tb >>= 4;
    };
    auto step = [&]() { reqB(); reqC(); wait_vm<13>(); BARL(); };
    u64 tt[11] = {0, 0, 0, 0, 0, 0, 0, 0, 0, 0, 0};
#pragma unroll 1
    for (int k = 2; k < 7; ++k) {
        reqH(q, smem, w, k); reqX(q, smem, w, k == 6 ? 10 : k);
        wait_vm<10>();
        BARL();
    }
    q.xg += 512 * 896;
    auto reqX2 = [&](int a, int b) {
#pragma unroll
        for (int i = a; i < b; ++i) reqX(q, smem, w, i == 6 ? 10 : i);
    };
    reqX2(0, N4);
    wait_vm<5 + 2 * N4>();
    BARL();
    reqX2(N4, N5);
    wait_vm<2 * N5>();
    BARL();
    TT(0);
    reqX2(N5, N6);
    unsigned own = (lane & 15) == pm, hv = 0, hw = 0;
    u64 m3 = 3u << (t3 << 1 & 14) | 3u << (t3 >> 3 & 14) | 3u << (t3 >> 7 & 14);
    asm volatile("" : "+v"(offA[0]), "+v"(offA[1]), "+v"(offA[2]), "+v"(offA[3]), "+v"(offB), "+v"(own));
    asm volatile("" : "+s"(mB), "+s"(m3), "+s"(abg), "+s"(cg), "+s"(lb0), "+s"(tb), "+s"(tc));
    if (w == 0) {
        const u32x2 t = *reinterpret_cast<const u32x2*>(smem + L_KEY + lane * 8);
        hs.off = t[0]; hs.snap = t[1];
    }
    wait_vm<2 * (N6 - KA)>();
    BARL();
    TT(1);
    reqX2(N6, 7);
    reqC();
    if (V1) reqC();
    wait_vm<4 + 4 * V1>();
    BARL();
    TT(2);
    if (w == 0) hs_add(hs, 1ull << pm);
    if (!V1) reqC();
#define PDA 5
    if (PDA && w == 0) { __builtin_amdgcn_s_sleep(PDA); hs_rmw(hs, hv, M_LO); }
    BARL();
    TT(3);
    if (w == 0) {
        if (!PDA) hs_rmw(hs, hv, M_LO);
        __builtin_amdgcn_s_sleep(PDG);
        hs_wait2(hs, m3, M_LO, own, hv, hw);
        *(volatile LDS_AS unsigned*)(smem + L_GO + 4) = 1u;
    } else lds_wait<SL1>(L_GO + 4);
    TT(4);
    reqB(); reqB();
    unsigned h2 = 0;
    if (w == 0) hs_rmw(hs, h2, M_LO);
    reqC();
    wait_vm<9>();
    asm volatile("" ::"v"(hv), "v"(hw));
    BARL();
    reqB(); reqC();
    {
        const unsigned v = hs_take<13>(h2);
        if (w == 0) { if (!hs_new(hs, v, M_LO)) hs_wait(hs, M_LO, M_LO, own); }
    }
    BARL();
    if (w == 0) hs_add(hs, 0x10001ull << pm | (duty ? M_OR : 0ull));
#pragma unroll 1
    for (int i = 1; i < 4; ++i) step();
    if (w == 0) hs_rmw(hs, h2, M_LO | M_HI, own);
    step();
    if (w == 0) { if (!hs_new(hs, hs_take<9>(h2), M_HI)) hs_wait(hs, M_HI, M_HI, 0u); }
#pragma unroll 1
    for (int i = 5; i < 13; ++i) step();
    WO wq;
    wq.g = wog;
    wo_init(wq, lane, w);
    reqB();
    if (duty) {
#pragma unroll
        for (int n = 0; n < 14; ++n) {
            if (n == WP1) { wait_vm<9 + WP1>(); BARL(); }
            if (n == WP2) { wait_vm<WP2>(); BARL(); }
            wo_req(wq, smem + W_0 + n * 4096 + w * 1024);
        }
    } else {
        wait_vm<9>(); BARL();
        wait_vm<0>(); BARL();
    }
    wait_vm<0>();
    BARL();
}
template <int NE, class F>
__device__ __forceinline__ void ab_mma(char* smem, unsigned dbase, unsigned sg, int lane, int sh, int ef, u16* ab, F post) {
    const int fr = lane & 15, fq = lane >> 4;
    const int z0 = fr * 128 + ((fq ^ (fr & 7)) * 16), z1 = z0 ^ 64;
    unsigned a0 = sh * 4096 + z0, a1 = sh * 4096 + z1, b0 = H_0 + ef * 2048 + z0, b1 = H_0 + ef * 2048 + z1;
    unsigned doff = dbase + (unsigned)(sh * 10240 + (ef * 16 + fr) * 64 + ((fq & 1) * 2 + (fq >> 1)) * 16);
    asm volatile("" : "+v"(a0), "+v"(a1), "+v"(b0), "+v"(b1), "+v"(doff));
    const char *sa0 = smem + a0, *sa1 = smem + a1, *sb0 = smem + b0, *sb1 = smem + b1;
    const __amdgpu_buffer_rsrc_t rs = __builtin_amdgcn_make_buffer_rsrc(ab, 0, 192 * B_SL, 0x00020000);
    f32x4 acc[NE][2];
    bf16x8 xf[2][2][2], hf[2][2][NE];
    auto rd = [&](int k, int i) {
        const int h = i >= 2 + NE, n = i - h * (2 + NE);
        if (n < 2) xf[k & 1][h][n] = lds_rd128((h ? sa1 : sa0) + (k == 6 ? 10 : k) * X_SL + n * 2048);
        else hf[k & 1][h][n - 2] = lds_rd128((h ? sb1 : sb0) + k * H_SL + (n - 2) * 2048);
    };
#pragma unroll 1
    for (int p = 0; p < 2; ++p) {
#pragma unroll
        for (int a = 0; a < NE; ++a) {
            acc[a][0] = f32x4{0.f, 0.f, 0.f, 0.f}; acc[a][1] = f32x4{0.f, 0.f, 0.f, 0.f};
            asm volatile("" : "+v"(acc[a][0]), "+v"(acc[a][1]));
        }
#pragma unroll
        for (int k = 0; k < 8; ++k) {
            if (k == 0) BARL();
            else if (k == KA) { if (p) wait_vm<0>(); BARL(); }
            else if (k == 7) { if (p) BAR(); }
            else if (!p) { if (k == 6) { post(); BAR(); } else BARL(); }
            if (k == 0 && p == 0) STAMP(0, 1);
            SB();
#pragma unroll
            for (int i = 0; i < 4 * NE; ++i) {
                const int h = i >= 2 * NE, n = i - h * 2 * NE;
                if (k == 7) acc[n >> 1][n & 1] = MFMA(xf[0][h][n & 1], hf[0][h][n >> 1], acc[n >> 1][n & 1]);
                else if (k) asm volatile("v_mfma_f32_16x16x32_bf16 %0, %1, %2, %0" : "+v"(acc[n >> 1][n & 1]) : "v"(xf[~k & 1][h][n & 1]), "v"(hf[~k & 1][h][n >> 1]));
                SB();
                if (k < 7 && i < 4 + 2 * NE) rd(k, i);
                SB();
            }
        }
#pragma unroll
        for (int mi = 0; mi < NE; ++mi) {
            const f32x4 a = acc[mi][0], b = acc[mi][1];
            auto x = __builtin_amdgcn_permlane16_swap(pk2(a[0], a[1]) ^ sg, pk2(b[0], b[1]) ^ sg, false, false);
            auto y = __builtin_amdgcn_permlane16_swap(pk2(a[2], a[3]) ^ sg, pk2(b[2], b[3]) ^ sg, false, false);
            st16_sc1(rs, doff + mi * 1024u, u32x4{x[0], y[0], x[1], y[1]});
        }
        SB();
        doff += B_SL;
    }
}
#define NK 1
#define OPW 16
template <int NF>
__device__ __forceinline__ void y_mma(char* smem, int lane, int cw, u16* px, unsigned pxb, int ks, u16* out, unsigned ob0, int d4, Hs& hs) {
    const int fr = lane & 15, fq = lane >> 4;
    const int mh = cw >> 1, nh = cw & 1;
    f32x4 acc[5][4];
#pragma unroll
    for (int a = 0; a < NF; ++a)
#pragma unroll
        for (int b = 0; b < 4; ++b) { acc[a][b] = f32x4{0.f, 0.f, 0.f, 0.f}; asm volatile("" : "+v"(acc[a][b])); }
    const int z0 = fr * 128 + ((fq ^ (fr & 7)) * 16), z1 = z0 ^ 64;
    unsigned a0 = 3 * A_SL + mh * 8192 + z0, a1 = 3 * A_SL + mh * 8192 + z1, b0 = B_R0 + nh * (NF * 2048) + z0, b1 = B_R0 + nh * (NF * 2048) + z1;
    unsigned pl = pxb + (unsigned)(cw * 8192 + lane * 16), ph = pl + (unsigned)(6144 - 512 - lane * 8);
    if (NF == 5) asm volatile("" : "+v"(ph));
    unsigned ob = ob0 + (unsigned)((mh * 64 + fr) * 896 + nh * 64 + (fq & 1) * 16 + (fq >> 1) * 8) * 2u;
    const unsigned one = fq == (fr >> 2) ? 0x3f80u << ((fr & 1) * 16) : 0u, d0 = (fr & 2) ? 0u : one, d1 = (fr & 2) ? one : 0u;
    u32x4 s0 = {d0, d1, 0u, 0u}, s1 = {0u, 0u, d0, d1};
    unsigned own = lane == 32 + ks;
    asm volatile("" : "+v"(a0), "+v"(a1), "+v"(b0), "+v"(b1), "+v"(pl), "+v"(ob), "+v"(s0), "+v"(s1), "+v"(own));
    const char *sa0 = smem + a0, *sa1 = smem + a1, *sb0 = smem + b0, *sb1 = smem + b1;
    const __amdgpu_buffer_rsrc_t rs = __builtin_amdgcn_make_buffer_rsrc(px, 0, 48 * 4 * 32768, 0x00020000);
    const __amdgpu_buffer_rsrc_t rs_out = __builtin_amdgcn_make_buffer_rsrc(out, 0, 2048 * 896 * 2, 0x00020000);
    bf16x8 cf[2][4], xf[2][NF];
    auto rdx = [&](int mi, int k, int st) { xf[k][mi] = lds_rd128((k ? sb1 : sb0) + st * B_SL + mi * 2048); };
    auto rdc = [&](int l, int k, int st) { cf[k][l] = lds_rd128((k ? sa1 : sa0) + st * A_SL + l * 2048); };
    auto mf = [&](int mi, int l, int k) {
        asm volatile("v_mfma_f32_16x16x32_bf16 %0, %1, %2, %0" : "+v"(acc[mi][l]) : "v"(cf[k][l]), "v"(xf[k][mi]));
        SB();
    };
    int da = -3 * A_SL;
    auto head = [&](int st, int lo) {
        int n = 0;
        __builtin_amdgcn_s_waitcnt(0xc47f);
        SB();
#pragma unroll
        for (int i = 4 * lo; i < 16; ++i) {
            mf(((i >> 1) ^ (i >> 2)) & 1, i >> 2, (i ^ (i >> 1)) & 1);
            if (n < 2 * NF - 4) rdx(2 + (n >> 1), n & 1, st);
            if (st == 2 && n == 8) sa0 += da;
            if (st == 2 && n == 10) { sa1 += da; da = -da; }
            ++n;
            SB();
        }
        if (NF == 5) {
#pragma unroll
            for (int i = 0; i < 8 - 2 * lo; ++i) mf(2, 3 - (i >> 1), (i ^ (i >> 1)) & 1);
        }
    };
    auto rest = [&](int st, int lo) {
#pragma unroll
        for (int i = 4 * lo; i < 16; ++i) {
            mf(NF - 2 + (((i >> 1) ^ (i >> 2)) & 1), i >> 2, (i ^ (i >> 1)) & 1);
            if ((i & 3) >= 2) rdc(i >> 2, ~i & 1, st);
            else if (i - 4 * lo < 8) rdx((i >> 2) - lo, i & 1, st);
            else if (lo) rdc(0, i & 1, st);
            SB();
        }
    };
    STAMP(0, 4);
    BARL();
    STAMP(0, 5); STAMPC(0, 3);
#pragma unroll
    for (int i = 0; i < 16; ++i) {
        if ((i & 3) >= 2) rdc(i >> 2, i & 1, 0);
        else if (i < 8) rdx(i >> 2, i & 1, 0);
        SB();
    }
#pragma unroll 1
    for (int t = 0; t < 4; ++t) {
        head(0, 0);
        wait_vm<0>();
        BARK();
        rest(1, 0); head(1, 0);
        BARK();
        rest(2, 0); head(2, 0);
        BARK();
        rest(0, 0);
    }
    bf16x8 kf[4];
    unsigned ka = 0, kb = 0;
    head(0, 0); BARK();
    rest(1, 0); head(1, 1); BARK();
    kf[0] = cf[0][0]; kf[1] = cf[1][0];
    rest(2, 1); head(2, 1); BARK();
    hs_rmw(hs, kb, M_XR);
    kf[2] = cf[0][0]; kf[3] = cf[1][0];
    rest(0, 1);
    STAMP(0, 18); STAMPC(0, 4);
    const int pw = ks * 32768;
    auto put = [&](int p, int h) {
        const int sl = 2 * (p - 1) + h;
        const f32x4 a = acc[h < 2 ? 2 * h : NF - 1][p], b = acc[h < 2 ? 2 * h + 1 : 0][p];
        if (h < 2) __builtin_amdgcn_raw_buffer_store_b128(u32x4{pk2(a[0], a[1]), pk2(a[2], a[3]), pk2(b[0], b[1]), pk2(b[2], b[3])}, rs, pl + (sl & 3) * 1024u, pw + (sl >> 2 << 12), 16);
        else __builtin_amdgcn_raw_buffer_store_b64(u32x2{pk2(a[0], a[1]), pk2(a[2], a[3])}, rs, ph + p * 512u, pw, 16);
    };
    constexpr int NG = 2 * NF - 4, NL = 12 + 3 * NG;
#pragma unroll
    for (int i = 0; i < NL + 2 * NF; ++i) {
        const int j = i - 12, l = i < 12 ? 1 + (i >> 2) : i < NL ? 1 + j / NG : 0, mi = i < 12 ? (i >> 1) & 1 : i < NL ? 2 + j % NG / 2 : (i - NL) >> 1;
        acc[mi][l] = MFMA(cf[i & 1][l], xf[i & 1][mi], acc[mi][l]);
        SB();
        if (i < NG) rdx(2 + (i >> 1), i & 1, 0);
        const int e = i - 2;
        if (e == 4 || e == 8 || e == 12) put(e >> 2, 0);
        if (e > 12 && e <= NL && (e - 12) % NG == 4 % NG) put((e - 13) / NG + 1, 1);
        if (NF == 5 && e > 12 && e <= NL && (e - 12) % NG == 0) put((e - 12) / NG, 2);
        if (i > NL + 1) rdx((i - NL - 2) >> 1, i & 1, 1);
        if (i == NL + 3) BARL();
        SB();
    }
    rdx(NF - 1, 0, 1); rdx(NF - 1, 1, 1);
#pragma unroll
    for (int i = 0; i < 4 * NF; ++i) {
        const int mi = (i >> 1) % NF;
        acc[mi][0] = MFMA(kf[(i >= 2 * NF) * 2 + (i & 1)], xf[i & 1][mi], acc[mi][0]);
        SB();
        if (i < 2 * NF) rdx(mi, i & 1, 2);
        SB();
    }
#pragma unroll
    for (int mi = 0; mi < NF; ++mi) asm volatile("" : "+v"(acc[mi][0]));
    STAMP(0, 19);
    const bool all = hs_new(hs, hs_take<0>(kb), M_XR);
    STAMP(0, 20);
    hs_rmw(hs, ka, M_XW, own);
    int pr[4];
#pragma unroll
    for (int l = 1; l < 4; ++l) pr[l - 1] = ((ks - l) & 3) * 32768 + (l == 3 ? 4096 : 0);
    pr[3] = pr[2] - 4096;
    unsigned pl2 = pl, ph2 = ph;
    u32x4 s01 = s0 | s1;
    __amdgpu_buffer_rsrc_t ro2 = rs_out;
    asm volatile("" : "+s"(pr[0]), "+s"(pr[1]), "+s"(pr[2]), "+s"(pr[3]), "+s"(ro2));
    asm volatile("" : "+v"(pl2), "+v"(ph2), "+v"(s01));
    unsigned o2 = ob - (unsigned)(fr & 8) * (1792 - 8), o4 = ob + (unsigned)(d4 - nh * 96);
    asm volatile("" : "+v"(o2), "+v"(o4));
    auto pack = [&](int pr) {
        const f32x4 a = acc[2 * pr][0], b = acc[pr < 2 ? 2 * pr + 1 : 2 * pr][0];
        auto x = __builtin_amdgcn_permlane16_swap(pk2(a[0], a[1]), pk2(b[0], b[1]), false, false);
        auto y = __builtin_amdgcn_permlane16_swap(pk2(a[2], a[3]), pk2(b[2], b[3]), false, false);
        return u32x4{x[0], y[0], x[1], y[1]};
    };
    u32x4 v0, v1;
    u32x2 o5 = {0u, 0u};
    {
        const f32x4 z = {0.f, 0.f, 0.f, 0.f};
#pragma unroll
        for (int h = 0; h < 2; ++h) {
            const f32x4 a = acc[2 * h][0], b = acc[2 * h + 1][0];
            const bf16x8 p = __builtin_bit_cast(bf16x8, u32x4{pk2(a[0], a[1]), pk2(a[2], a[3]), pk2(b[0], b[1]), pk2(b[2], b[3])});
            acc[2 * h][0] = MFMA(p, __builtin_bit_cast(bf16x8, s0), z);
            acc[2 * h + 1][0] = MFMA(p, __builtin_bit_cast(bf16x8, s1), z);
            asm volatile("" : "+v"(acc[2 * h][0]), "+v"(acc[2 * h + 1][0]));
        }
        if (NF == 5) { const f32x4 a = acc[4][0]; o5 = u32x2{pk2(a[0], a[1]), pk2(a[2], a[3])}; acc[4][0] = z; asm volatile("" : "+v"(o5), "+v"(acc[4][0])); }
    }
    __builtin_amdgcn_s_sleep(PD2 - 2);
    hs_wait2(hs, M_XW ^ 1ull << (32 + ks), M_XW ^ 1ull << (32 + ks), 0u, ka, kb);
    STAMP(1, 0);
    bf16x8 pv[2][3];
    u32x2 p5[3];
#pragma unroll
    for (int h = 0; h < (NF + 1) / 2; ++h)
#pragma unroll
        for (int l = 1; l < 4; ++l) {
            const int sl = 2 * (l - 1) + h;
            if (h < 2) pv[h][l - 1] = __builtin_bit_cast(bf16x8, __builtin_amdgcn_raw_buffer_load_b128(rs, pl2 + (sl & 3) * 1024u, pr[l - 1], 16));
            else p5[l - 1] = __builtin_amdgcn_raw_buffer_load_b64(rs, ph2 + l * 512u, pr[l == 3 ? 3 : l - 1], 16);
            SB();
        }
    if (!all) hs_add(hs, 1ull << (32 + ks));
#pragma unroll
    for (int h = 0; h < (NF + 1) / 2; ++h) {
        if (h < 2) {
#pragma unroll
            for (int l = 0; l < 3; ++l) {
                acc[2 * h][0] = MFMA(pv[h][l], __builtin_bit_cast(bf16x8, s0), acc[2 * h][0]);
                acc[2 * h + 1][0] = MFMA(pv[h][l], __builtin_bit_cast(bf16x8, s1), acc[2 * h + 1][0]);
                SB();
                if (h == 1 && l == 1) { v0 = pack(0); asm volatile("" : "+v"(v0)); SB(); }
            }
        } else {
            acc[4][0] = MFMA(__builtin_bit_cast(bf16x8, (u32x4{p5[0][0], p5[0][1], p5[1][0], p5[1][1]})), __builtin_bit_cast(bf16x8, s01), acc[4][0]);
            acc[4][0] = MFMA(__builtin_bit_cast(bf16x8, (u32x4{p5[2][0], p5[2][1], o5[0], o5[1]})), __builtin_bit_cast(bf16x8, s01), acc[4][0]);
            SB();
        }
    }
    STAMP(1, 1);
    if (NK) asm volatile("" ::"v"(ka), "v"(kb), "v"(acc[NF - 1][0]));
    v1 = pack(1);
    {
        u32x4 p0, p1;
#pragma unroll
        for (int i = 0; i < 4; ++i) p0[i] = __builtin_amdgcn_update_dpp(v0[i], v1[i], 0x128, 0xf, 0xc, false);
#pragma unroll
        for (int i = 0; i < 4; ++i) p1[i] = __builtin_amdgcn_update_dpp(v1[i], v0[i], 0x128, 0xf, 0x3, false);
        st16_out<NF == 5 ? OPW : 16>(ro2, o2, p0);
        st16_out<NF == 5 ? OPW : 16>(ro2, o2 + 8 * 1792u, p1);
    }
    SB();
#define OP4 16
    if (NF == 5 && OP4 >= 0) { const u32x4 v = pack(2); if (!(fq & 1)) st16_out<OP4 & 31>(ro2, o4, v); }
    if (!NK) hs_keep(ka, kb);
}

constexpr int Q_ASL = 18432, Q_WR = 3 * Q_ASL, Q_WSL = 21504, GP = 1216, Q_DUMMY = 162816, W_A = Q_DUMMY - 14 * 4096, Q_TAB = 84 * GP;
__device__ __forceinline__ void qkv_reqW(char* smem, const char* wg, unsigned off, int c, int kw, int sw) {
    const char* g = wg + (long)kw * (32 * 2688);
    const char* sb = smem + Q_WR + sw * Q_WSL + c * 1792;
#pragma unroll
    for (int k = 0; k < 6; ++k) glds16m(g + (k & 1) * (8 * 2688) + (k >> 1) * 896, off, sb + (k & 1) * 896 + (k >> 1) * 7168, 0xffffffffffffffull);
}
struct QX { unsigned off; int r0, r1, r2; };
__device__ __forceinline__ QX qkv_offX(int c, int lane, int s0) {
    QX q;
    q.off = (unsigned)((lane >> 3) * 1792 + ((lane & 7) ^ (lane >> 3)) * 16);
    q.r0 = max(s0 + (c < 2 ? c - 16 : 16 * c - 32), 0) * 896;
    q.r1 = (s0 + 16 * c + 32) * 896;
    q.r2 = q.r1 + (c < 2 ? 64 : -63) * 896;
    return q;
}
__device__ __forceinline__ void qkv_reqX(char* smem, const u16* __restrict__ xA, const QX& q, int c, int kx, int sx) {
    const char* g = (const char*)xA + kx * 128;
    const char* sb = smem + sx * Q_ASL + c * 1024;
    glds16a(g + q.r0, q.off, sb);
    glds16a(g + q.r1, q.off, sb + 4096);
    glds16a(g + q.r2, q.off, sb + 2 * 4096);
    glds16a(g + q.r1 + 896, q.off, sb + 3 * 4096);
    if (c < 2) glds16a(g + q.r1 + 65 * 896, q.off, sb + 4 * 4096);
}
template <int V> struct QS {
    static constexpr int col(int i) { constexpr int t[3][6] = {{0, 1, 2, 5, 3, 4}, {2, 3, 1, 0, 9, 10}, {0, 1, 2, 2, 3, 4}}; return t[V][i]; }
    static constexpr int wof(int i) { return V == 1 ? ((col(i) + 5) / 7) * 7168 + ((col(i) + 5) % 7 - 5) * 32 : col(i) * 32; }
    static constexpr int lo(int i) { return V == 0 ? (i == 3 ? 3 : 1) : V == 1 ? (i == 2 || i == 3 ? 1 : 0) : 0; }
    static constexpr int hi(int i) { return i != 3 || V == 0 ? 9 : V == 1 ? 3 : 0; }
};
#define RS2 0
#define LB 1
#define LC 3
#define LW 4
template <int V>
__device__ __forceinline__ void qkv_mma(char* smem, int lane, int G0, u64 (&tt)[11]) {
    typedef QS<V> S;
    const int fr = lane & 15, fq = lane >> 4;
    const int u = fq & 1, ah = fq >> 1, q = (lane >> 2) & 3, p = lane & 3;
    constexpr int R0 = V == 0 ? 1 : 0;
    f32x4 acc[6][9];
    unsigned aw = Q_WR + Q_WSL + (16 * ah + 4 * u + q) * 224 + 8 * p + (V < 2 ? G0 * 32 : (G0 >> 3) * 7168 + 64);
    unsigned ax0 = fr * 128 + ((fq ^ (fr & 7)) * 16), ax1 = ax0 ^ 64;
    unsigned ai = (G0 * 4 + fq) * GP + fr * 8, ah0 = ai + (fr & 8) * 64;
#pragma unroll
    for (int i = 0; i < 6; ++i)
#pragma unroll
        for (int r = S::lo(i); r < S::hi(i); ++r) { acc[i][r] = f32x4{0.f, 0.f, 0.f, 0.f}; asm volatile("" : "+v"(acc[i][r])); }
    asm volatile("" : "+v"(aw), "+v"(ax0), "+v"(ax1), "+v"(ai));
    if (V) asm volatile("" : "+v"(ah0));
    const char *pw = smem + aw, *px0 = smem + ax0, *px1 = smem + ax1;
    bf16x8 xf[9];
    s16x4 wl[6], wh[6];
    auto rdw = [&](int i, int h) {
        if (S::hi(i) == 0) return;
        if (h) wh[i] = lds_tr(pw + S::wof(i) + 8 * 224); else wl[i] = lds_tr(pw + S::wof(i));
    };
    int cw = 0x2108, cx = 0x24, dw = 0, dx = 0;
    auto tick = [&](int kind, bool extra) {
#pragma unroll
        for (int g = 0; g < 3; ++g) {
            if (g == 2 && (kind == 1 || extra)) BARK();
            int n = 0;
#pragma unroll
            for (int r = 0; r < 9; ++r)
#pragma unroll
                for (int e = 0; e < 2; ++e) {
                    const int i = 2 * g + (e ^ (r & 1));
                    if (r < S::lo(i) || r >= S::hi(i)) { if (g == 2 && e == 1 && r >= R0) { xf[r] = lds_rd128((kind ? px0 : px1) + r * 2048); SB(); } continue; }
                    asm volatile("v_mfma_f32_16x16x32_bf16 %0, %1, %2, %0" : "+v"(acc[i][r]) : "v"(cat(wl[i], wh[i])), "v"(xf[r]));
                    SB();
                    if (g < 2 ? n < 4 : (n < 8 && !(n & 1))) { const int m = g < 2 ? n : n >> 1; rdw(2 * ((g + 1) % 3) + (m >> 1), m & 1); }
                    if (g == 2 && e == 1) xf[r] = lds_rd128((kind ? px0 : px1) + r * 2048);
                    if (g == 0) {
                        if (n == 4) { dw = cw & 1 ? -4 * Q_WSL : Q_WSL; asm volatile("" : "+s"(dw)); }
                        if (n == 6) { cw >>= 1; asm volatile("" : "+s"(cw)); }
                        if (n == 8 && kind) { dx = cx & 1 ? -2 * Q_ASL : Q_ASL; asm volatile("" : "+s"(dx)); }
                        if (n == 10 && kind) { cx >>= 1; asm volatile("" : "+s"(cx)); px0 += dx; }
                        if (n == 12 && kind) px1 += dx;
                    }
                    if (g == 1 && n == 5) pw += dw;
                    ++n;
                    SB();
                }
        }
    };
    auto last = [&]() {
        int fi[48], fw[48], nf = 0, na = 99;
        u32x2 cv[48];
        auto behind = [&](int f) {
            const int a = f - LW, b = f - LC;
            if (a >= 0 && a < nf) {
                const int r = fw[a];
                *reinterpret_cast<u32x2*>(smem + (r ? ai : ah0) + S::col(fi[a]) * 4 * GP + (r == 0 ? 0 : r < 5 ? 128 * r - 64 : 128 * r)) = cv[a];
                SB();
            }
            if (b >= 0 && b < nf) { const f32x4 d = acc[fi[b]][fw[b]]; cv[b] = u32x2{pk2(d[0], d[1]), pk2(d[2], d[3])}; asm volatile("" : "+v"(cv[b])); }
            if (a == na + LB) { SB(); BARK(); }
        };
#pragma unroll
        for (int h = 0; h < (RS2 ? 6 : 3); ++h) {
            const int g = h % 3;
            int n = 0;
            if (h == 3) na = nf - 1;
#pragma unroll
            for (int r = 0; r < 9; ++r)
#pragma unroll
                for (int e = 0; e < 2; ++e) {
                    const int i = 2 * g + e;
                    if (r < S::lo(i) || r >= S::hi(i) || (RS2 && (0x67 >> r & 1) != (h < 3))) continue;
                    asm volatile("v_mfma_f32_16x16x32_bf16 %0, %1, %2, %0" : "+v"(acc[i][r]) : "v"(cat(wl[i], wh[i])), "v"(xf[r]));
                    SB();
                    if (n < 2 && h < 2) { rdw(2 * h + 2 + n, 0); rdw(2 * h + 2 + n, 1); }
                    fi[nf] = i; fw[nf] = r; ++nf;
                    behind(nf - 1);
                    ++n;
                    SB();
                }
        }
        asm volatile("s_nop 7");
#pragma unroll
        for (int f = 0; f < LW; ++f) { behind(nf + f); SB(); }
    };
    TT(9);
    __builtin_amdgcn_s_setprio(LP);
    BARL();
    TT(0); TT(5);
#pragma unroll
    for (int r = R0; r < 9; ++r) {
        if (r - R0 < 4) { rdw((r - R0) >> 1, (r - R0) & 1); SB(); }
        xf[r] = lds_rd128(px0 + r * 2048);
        SB();
    }
#pragma unroll 1
    for (int s = 0; s < 6; ++s) { tick(0, s == 0); tick(1, false); }
    TT(1); TT(6);
    tick(0, false);
    BARK();
    TT(2);
    last();
    TT(3); TT(7);
}

struct AMk { f32x4 c0, c1; u32x4 m; };
__device__ __forceinline__ AMk attn_mask(unsigned ln, bool top) {
    AMk t;
    const int out = (int)(~((0x1feu << (ln & 15)) & (top ? ~0xffu : ~0u)) >> (4 * (ln >> 4)));
    unsigned b0[4], b1[4];
#pragma unroll
    for (int i = 0; i < 4; ++i) {
        b0[i] = (unsigned)__builtin_amdgcn_sbfe(out, i, 1); b1[i] = (unsigned)__builtin_amdgcn_sbfe(out, 16 + i, 1);
        t.c0[i] = __uint_as_float(b0[i] & 0xf149f2cau);
        t.c1[i] = __uint_as_float(b1[i] & 0xf149f2cau);
    }
#pragma unroll
    for (int i = 0; i < 2; ++i) {
        t.m[i] = ~((b0[2 * i] & 0xffffu) | (b0[2 * i + 1] & 0xffff0000u));
        t.m[2 + i] = ~((b1[2 * i] & 0xffffu) | (b1[2 * i + 1] & 0xffff0000u));
    }
    asm volatile("" : "+v"(t.c0), "+v"(t.c1), "+v"(t.m));
    return t;
}
struct AA { u32x4 a; u32x2 b; };
__device__ __forceinline__ AA attn_addr(unsigned ln, int w, int rt, int hg) {
    AA t;
    const unsigned fr = ln & 15, fq = ln >> 4, b = (w >> 1) * 16, z = (w & 1) * 576, gq = ((fq & 1) * 2 + (fq >> 1)) * GP + z, gv = (56 + (ln & 3)) * GP + z;
    t.a[0] = gq + (b + fr) * 8;
    t.a[1] = fq == 3 ? 1152 - 64 : t.a[0];
    t.a[2] = gq + (b + 16 + min(fr, 7u)) * 8;
    t.a[3] = gv + (b + (ln >> 2)) * 8;
    t.b[0] = gv + (b + 16 + min(ln >> 2, 7u)) * 8;
    t.b[1] = (unsigned)(rt * 56 + hg * 14 + fq) * 2048u + (w * 16 + fr) * 16u;
    asm volatile("" : "+v"(t.a), "+v"(t.b));
    return t;
}
#define Q5 tp(0)
#define Q5N 1
#define Q6 tp(1)
#define Q7 tp(2); tp(3)
#define Q8 tw()
#define QD 0
__device__ __forceinline__ unsigned qkv_attn(char* smem, const u16* xA, const u16* wqkv, const char* wo, char* ws, int rt, int hg, int tid, int w,
                                         Hs& hs, int kst, int ro, int co, u64 cb, int qi, WO& wq) {
    const int lane = tid & 63, s0 = rt * 128, c = w & 3;
    auto st = [&](int i) { const int k = kst + i; return k >= 7 ? k - 7 : k; };
    u16* o = (u16*)(ws + WS_O);
    AMk am;
    AA aa;
    unsigned hv2 = 0;
    u64 tt[11] = {0, 0, 0, 0, 0, 0, 0, 0, 0, 0, 0};
    if (w >= 4) {
        if (w == 4) qkv_mma<0>(smem, lane, 0, tt);
        else if (w == 5) qkv_mma<1>(smem, lane, 5, tt);
        else qkv_mma<2>(smem, lane, 7 * w - 33, tt);
        __builtin_amdgcn_s_setprio(0);
        const char* tb = smem + W_A + c * 1024 + lane * 16;
        aa.a = *reinterpret_cast<const u32x4*>(tb); aa.b = *reinterpret_cast<const u32x2*>(tb + 4096);
        tb = smem + Q_TAB + lane * 16;
        am.c0 = *reinterpret_cast<const f32x4*>(tb); am.c1 = *reinterpret_cast<const f32x4*>(tb + 1024);
        am.m = *reinterpret_cast<const u32x4*>(tb + 2048);
        BAR();
        TT(4);
    } else {
#define LP0 1
        __builtin_amdgcn_s_setprio(LP0);
        const char* wg = (const char*)wqkv + ((c >> 1) * 16 + (c & 1) * 4) * 2688 + hg * 224;
        const unsigned offW = (unsigned)(lane * 16 + (lane * 37 >> 9) * (2688 - 224));
        if (QD) __builtin_amdgcn_s_sleep(QD);
        TT(0);
#define XF 1
        if (!XF) { qkv_reqW(smem, wg, offW, c, 2 * kst, 1); SB(); }
        const QX qx = qkv_offX(c, lane, s0);
        qkv_reqX(smem, xA, qx, c, kst, 0);
        if (XF) { SB(); qkv_reqW(smem, wg, offW, c, 2 * kst, 1); }
        TT(1);
        qkv_reqW(smem, wg, offW, c, 2 * kst + 1, 2);
        TT(2);
        wait_vm<6>();
        TT(4);
        BARL();
        __builtin_amdgcn_s_setprio(0);
        STAMP(0, 1);
        unsigned hv = 0;
        if (w == 0) { hs.off = hs_off(lane, HS_O + rt * 256, 0, ro, co); hs_rmw(hs, hv, M_R14 | M_OW); }
#define XO 0
        const bool late = XO == 1 || (XO == 2 && ((hg + 1) & 2));
        if (!late) qkv_reqX(smem, xA, qx, c, st(1), 1);
        qkv_reqW(smem, wg, offW, c, 2 * st(1), 3);
        if (late) wait_vm<6>(); else wait_vm<10>();
        BARL();
        STAMP(0, 9);
        int sa = 4, sx = late ? 1 : 2;
        const unsigned tc = (unsigned)((c * 32 + (lane >> 1)) * 4096), ts = (lane & 1) * 4;
        const unsigned tb = (TQ + qi * 0x11111111u) & 0x77777777u | 0x88888888u;
        auto tp = [&](int i) { asm volatile("s_mov_b32 m0, %2\n\ts_nop 0\n\tglobal_load_lds_dword %0, %1" ::"v"(tc + ((tb >> 8 * i >> ts & 15) << 7)), "s"(cb), "i"(Q_DUMMY) : "memory"); };
#pragma unroll 1
        for (int s = 0; s < 5; ++s) {
            if (late) qkv_reqX(smem, xA, qx, c, st(s + 1), sx);
            qkv_reqW(smem, wg, offW, c, 2 * st(s + 1) + 1, sa);
            qkv_reqW(smem, wg, offW, c, 2 * st(s + 2), sa == 4 ? 0 : sa + 1);
            if (late) wait_vm<6>();
            else {
                qkv_reqX(smem, xA, qx, c, st(s + 2), sx);
                if (c < 2) wait_vm<11>(); else wait_vm<10>();
            }
            sa = sa >= 3 ? sa - 3 : sa + 2; sx = sx == 2 ? 0 : sx + 1;
            if (w == 0 && s == 3) { hs.snap = hs_take<63>(hv); hs_add(hs, M_OR); }
            BARL();
        }
        if (late) qkv_reqX(smem, xA, qx, c, st(6), 0);
        qkv_reqW(smem, wg, offW, c, 2 * st(6) + 1, 4);
        int l2 = lane;
        Q5;
        asm volatile("" : "+v"(l2));
        __builtin_assume(l2 >= 0 && l2 < 64);
        am = attn_mask(l2, rt + (w >> 1) == 0);
        aa = attn_addr(l2, w, rt, hg);
        const AA aa2 = attn_addr(l2, w + 4, rt, hg);
        unsigned wt = ((qi * 8 + w) * 49 + min(l2, 48)) * 128;
        asm volatile("" : "+v"(wt));
        auto tw = [&]() {
            asm volatile("s_mov_b32 m0, %2\n\ts_nop 0\n\tglobal_load_lds_dword %0, %1" ::"v"(wt), "s"(wo), "i"(Q_DUMMY) : "memory");
            asm volatile("s_mov_b32 m0, %2\n\ts_nop 0\n\tglobal_load_lds_dword %0, %1" ::"v"(wt), "s"(wo + 4 * 49 * 128), "i"(Q_DUMMY) : "memory");
        };
        wait_vm<Q5N>();
        BARL();
        Q6;
        {
            char* tq = smem + W_A + c * 1024 + l2 * 16;
            *reinterpret_cast<u32x4*>(tq) = aa2.a; *reinterpret_cast<u32x2*>(tq + 4096) = aa2.b;
            if (w == 3) {
                tq = smem + Q_TAB + l2 * 16;
                *reinterpret_cast<f32x4*>(tq) = am.c0; *reinterpret_cast<f32x4*>(tq + 1024) = am.c1; *reinterpret_cast<u32x4*>(tq + 2048) = am.m;
            }
        }
        BAR();
        if (w == 3) *reinterpret_cast<u32x2*>(smem + ((l2 & 3) * 7 + 4) * GP + 1152) = u32x2{0u, 0u};
        Q7;
        if (w == 0) hs_rmw(hs, hv2, M_R14);
        TT(3);
        BAR();
        Q8;
    }
    const unsigned m0[2] = {am.m[0], am.m[1]}, m1[2] = {am.m[2], am.m[3]};
    const f32x4 c0 = am.c0, c1 = am.c1;
    const float scale = 0.18898223650461361f, log2e = 1.4426950408889634f;
#define FTA(i)
    FTA(0);
    auto rd64 = [&](unsigned a, int g) { return *reinterpret_cast<const s16x4*>(smem + a + g * GP); };
    bf16x8 qf[4], k0[4], k1[4], v0[4], v1[4];
#pragma unroll
    for (int hh = 0; hh < 4; ++hh) {
        qf[hh] = cat(rd64(aa.a[0] + 64, 7 * hh), rd64(aa.a[1] + 64, 7 * hh + 4));
        k0[hh] = cat(rd64(aa.a[0], 28 + 7 * hh), rd64(aa.a[0], 32 + 7 * hh));
        k1[hh] = cat(rd64(aa.a[2], 28 + 7 * hh), rd64(aa.a[2], 32 + 7 * hh));
        SB();
    }
#pragma unroll
    for (int hh = 0; hh < 4; ++hh) {
        v0[hh] = cat(lds_tr(smem + aa.a[3] + 7 * hh * GP), lds_tr(smem + aa.b[0] + 7 * hh * GP));
        v1[hh] = cat(lds_tr(smem + aa.a[3] + (7 * hh + 4) * GP), lds_tr(smem + aa.b[0] + (7 * hh + 4) * GP));
    }
    SB();
    FTA(1);
#define AB2 1
    if (RS2 && AB2 == 0 && w < 4) { SB(); BARL(); SB(); }
    float l[2][4], mx[2];
#pragma unroll
    for (int hp = 0; hp < 2; ++hp) {
        const f32x4 a0 = MFMA(k0[2 * hp], qf[2 * hp], c0), a1 = MFMA(k1[2 * hp], qf[2 * hp], c1);
        const f32x4 b0 = MFMA(k0[2 * hp + 1], qf[2 * hp + 1], c0), b1 = MFMA(k1[2 * hp + 1], qf[2 * hp + 1], c1);
        float t[4];
#pragma unroll
        for (int i = 0; i < 4; ++i) {
            auto r = __builtin_amdgcn_permlane32_swap(__float_as_uint(vmax(a0[i], a1[i])), __float_as_uint(vmax(b0[i], b1[i])), false, false);
            t[i] = vmax(__uint_as_float(r[0]), __uint_as_float(r[1]));
        }
#pragma unroll
        for (int i = 0; i < 2; ++i) {
            unsigned a = pk2(t[2 * i], t[2 * i + 1]);
            a = pk2(__uint_as_float(a << 16) * scale, __uint_as_float(a & 0xffff0000u) * scale);
            l[hp][2 * i] = __uint_as_float(a << 16);
            l[hp][2 * i + 1] = __uint_as_float(a & 0xffff0000u);
        }
        mx[hp] = vmax(vmax3(l[hp][0], l[hp][1], l[hp][2]), l[hp][3]);
    }
    {
        auto r = __builtin_amdgcn_permlane16_swap(__float_as_uint(mx[0]), __float_as_uint(mx[1]), false, false);
        const float c = vmax(__uint_as_float(r[0]), __uint_as_float(r[1])) * -log2e;
        auto q2 = __builtin_amdgcn_permlane16_swap(__float_as_uint(c), __float_as_uint(c), false, false);
        mx[0] = __uint_as_float(q2[0]); mx[1] = __uint_as_float(q2[1]);
    }
    if (RS2 && AB2 == 1 && w < 4) { SB(); BARL(); SB(); }
    float sm[2];
#pragma unroll
    for (int hp = 0; hp < 2; ++hp) {
#pragma unroll
        for (int i = 0; i < 4; ++i) l[hp][i] = __builtin_amdgcn_exp2f(__builtin_fmaf(l[hp][i], log2e, mx[hp]));
        sm[hp] = (l[hp][0] + l[hp][1]) + (l[hp][2] + l[hp][3]);
    }
    {
        auto r = __builtin_amdgcn_permlane16_swap(__float_as_uint(sm[0]), __float_as_uint(sm[1]), false, false);
        const float c = __builtin_amdgcn_rcpf(__uint_as_float(r[0]) + __uint_as_float(r[1]));
        auto q2 = __builtin_amdgcn_permlane16_swap(__float_as_uint(c), __float_as_uint(c), false, false);
        sm[0] = __uint_as_float(q2[0]); sm[1] = __uint_as_float(q2[1]);
    }
    FTA(2);
    const f32x4 z = {0.f, 0.f, 0.f, 0.f};
    const __amdgpu_buffer_rsrc_t rs_o = __builtin_amdgcn_make_buffer_rsrc(o, 0, 2048 * 448 * 2, 0x00020000);
    const bool top = aa.a[0] != aa.a[1];
#pragma unroll
    for (int hp = 0; hp < 2; ++hp) {
        const unsigned p01 = pk2(l[hp][0] * sm[hp], l[hp][1] * sm[hp]), p23 = pk2(l[hp][2] * sm[hp], l[hp][3] * sm[hp]);
        auto e01 = __builtin_amdgcn_permlane32_swap(p01, p01, false, false);
        auto e23 = __builtin_amdgcn_permlane32_swap(p23, p23, false, false);
        u32x4 ov[2];
#pragma unroll
        for (int e = 0; e < 2; ++e) {
            const int hh = 2 * hp + e;
            u32x4 pw = {e01[e] & m0[0], e23[e] & m0[1], e01[e] & m1[0], e23[e] & m1[1]};
            const bf16x8 pf = __builtin_bit_cast(bf16x8, pw);
            const f32x4 o0 = MFMA(v0[hh], pf, z), o1 = MFMA(v1[hh], pf, z);
            ov[e] = u32x4{pk2(o0[0], o0[1]), pk2(o0[2], o0[3]), pk2(o1[0], o1[1]), pk2(o1[2], o1[3])};
        }
        ov[0][2] = top ? ov[1][0] : ov[0][2]; ov[0][3] = top ? ov[1][1] : ov[0][3];
        __builtin_amdgcn_raw_buffer_store_b128(ov[0], rs_o, aa.b[1], hp * 7 * 2048, 16);
        if (!top) __builtin_amdgcn_raw_buffer_store_b128(ov[1], rs_o, aa.b[1], (hp * 7 + 4) * 2048, 16);
    }
    FTA(4);
    if (w >= 4) {
        int l2 = lane;
        asm volatile("" : "+v"(l2));
        __builtin_assume(l2 >= 0 && l2 < 64);
        wo_init(wq, l2, c);
#pragma unroll 1
        for (int t = 0; t < WE; ++t) wo_req(wq, smem + W_A + t * 4096 + c * 1024);
    } else wait_vm<0>();
    const unsigned hv3 = hs_take<WE>(hv2);
    FTA(5);
    BARL();
    return hv3;
}
__device__ __forceinline__ void oproj(char* smem, u16* __restrict__ o, u16* __restrict__ out, int rt, int rg, int ct, int lane, unsigned ka, unsigned kb) {
    const int fr = lane & 15, fq = lane >> 4;
    const __amdgpu_buffer_rsrc_t rs_o = __builtin_amdgcn_make_buffer_rsrc(o, 0, 2048 * 448 * 2, 0x00020000);
    const unsigned ga = (unsigned)(rt * 56 + fq) * 2048u + (unsigned)(rg * 16 + fr) * 16u;
    bf16x8 fa[14];
#pragma unroll
    for (int ks = 0; ks < 14; ++ks) fa[ks] = __builtin_bit_cast(bf16x8, __builtin_amdgcn_raw_buffer_load_b128(rs_o, ga + ks % 7 * 8192u, ks / 7 * 57344, 16));
    SB();
    const int q = (lane >> 2) & 3, p = lane & 3, s = 2 * (fq & 1) + (q >> 1);
    const char* aw[4];
#pragma unroll
    for (int c = 0; c < 4; ++c) aw[c] = smem + (8 * fq + q) * 128 + ((c ^ s) * 32) + 8 * p;
    f32x4 acc[4];
#pragma unroll
    for (int c = 0; c < 4; ++c) acc[c] = f32x4{0.f, 0.f, 0.f, 0.f};
    constexpr int OA = 9;
    bf16x8 fb[14][4];
#pragma unroll
    for (int ks = 0; ks < 14 + OA; ++ks) {
        if (ks >= OA) {
#pragma unroll
            for (int c = 0; c < 4; ++c) acc[c] = MFMA(fb[ks - OA][c], fa[ks - OA], acc[c]);
        }
        if (ks < 14) {
#pragma unroll
            for (int c = 0; c < 4; ++c) fb[ks][c] = cat(lds_tr(aw[c] + ks * 4096), lds_tr(aw[c] + ks * 4096 + 512));
        }
        SB();
    }
    STAMP(1, 3);
    if (NK) asm volatile("" ::"v"(ka), "v"(kb), "v"(acc[3]));
    const __amdgpu_buffer_rsrc_t rs_out = __builtin_amdgcn_make_buffer_rsrc(out, 0, 2048 * 896 * 2, 0x00020000);
    const unsigned ob = (unsigned)((rt * 128 + (rg >> 1) * 32 + 2 * (fr & 7) + (rg & 1)) * 896 + ct * 64 + (fr & 8) * 4 + (fq & 1) * 16 + (fq >> 1) * 8) * 2u;
    u32x4 v[2];
#pragma unroll
    for (int pr = 0; pr < 2; ++pr) {
        const f32x4 a = acc[2 * pr], b = acc[2 * pr + 1];
        auto x = __builtin_amdgcn_permlane16_swap(pk2(a[0], a[1]), pk2(b[0], b[1]), false, false);
        auto y = __builtin_amdgcn_permlane16_swap(pk2(a[2], a[3]), pk2(b[2], b[3]), false, false);
        v[pr] = u32x4{x[0], y[0], x[1], y[1]};
    }
    u32x4 p0, p1;
#pragma unroll
    for (int i = 0; i < 4; ++i) {
        p0[i] = __builtin_amdgcn_update_dpp(v[0][i], v[1][i], 0x128, 0xf, 0xc, false);
        p1[i] = __builtin_amdgcn_update_dpp(v[1][i], v[0][i], 0x128, 0xf, 0x3, false);
    }
    st16_out<16>(rs_out, ob, p0);
    st16_out<16>(rs_out, ob + 16 * 1792u, p1);
}

__global__ __launch_bounds__(512, 2) void kf(const u16* xA, const u16* wqkv, const char* xB, const char* Hr, const char* Hi, const char* Cr,
                                          const char* Ci, const char* wo, u16* out, char* ws) {
    __shared__ __attribute__((aligned(16))) char smem[LDS_TOTAL];
    int tid = threadIdx.x, bid = blockIdx.x;
    u64 pc0;
    {
        unsigned part, off;
        asm volatile("s_getpc_b64 %0\n\ts_bfe_u32 %1, %2, 0x30003\n\ts_mulk_i32 %1, %3" : "=&s"(pc0), "=&s"(part), "+s"(bid) : "i"(CODE_PART) : "scc");
        asm volatile("s_mov_b32 m0, %4\n\tv_mad_u32_u24 %0, %1, 12, %2\n\tglobal_load_lds_dword %0, %3" : "=&v"(off), "+v"(tid) : "s"(part), "s"(pc0), "i"(Q_DUMMY) : "memory");
        asm volatile("" : "+s"(bid));
    }
    __builtin_assume(tid >= 0 && tid < 512 && bid >= 0 && bid < 256);
    const int lane = tid & 63, xcd = bid & 7;
    int w = __builtin_amdgcn_readfirstlane(tid >> 6), j = bid >> 3;
    STAMP(0, 0);
    const int ks = xcd & 3, rl = j & 3, c6 = j >> 2, r8 = (xcd >> 2) * 4 + rl;
    BL q;
    if (j < 24 && w < 4) {
        unsigned ox = (unsigned)(((ks & 1) * 1024 + r8 * 64) * 896), oh = (unsigned)((c6 + (c6 >= 2)) * (64 * 896));
#define KR 1
        q.kc = c6 + KR * rl >= 7 ? c6 + KR * rl - 7 : c6 + KR * rl;
        b_off(q, lane, w);
        q.h4 = ((0x5503ad >> 4 * c6 & 15) - 5) * (16 * 896);
        const char *p0 = xB, *p1 = Hr, *p2 = Hi;
        asm volatile("" : "+v"(q.offX), "+v"(q.offH));
        asm volatile("" : "+s"(p0), "+s"(p1), "+s"(p2), "+s"(ox), "+s"(oh), "+s"(q.kc), "+s"(q.h4));
        q.xg = p0 + ox;
        q.hg = ((ks >> 1) ? p2 : p1) + oh;
        STAMP(0, 7);
        reqH(q, smem, w, 0); reqX(q, smem, w, 0);
        reqH(q, smem, w, 1); reqX(q, smem, w, 1);
        asm volatile("" ::"s"(xA), "s"(wqkv), "s"(Cr), "s"(Ci), "s"(wo), "s"(out), "s"(ws));
    }
    asm volatile("" : "+s"(w), "+s"(j));
    __builtin_assume(w >= 0 && w < 8 && j >= 0 && j < 32);
    if (w == 7) *reinterpret_cast<u32x2*>(smem + L_GO) = u32x2{0u, 0u};
    Hs hs;
    hs.base = (const unsigned*)(ws + WS_HS); hs.off = 0; hs.snap = 0;
    int ro = 0, rg = 0, ct = 0;
    bool duty = true;
    u64 again = 0, mr = M_R14;
    unsigned ka = 0, kb = 0;
    int wb = W_0;
    if (j < 24) {
        u16* ab = (u16*)ws;
        u16* px = (u16*)(ws + WS_PX);
        const int rbl = j & 7, cg = j >> 3, rb = (xcd >> 2) * 8 + rbl;
#define DU 1
        duty = DU ? (j & 19) != 3 : j >= 4;
        const int un = DU ? j - min(c6, 4) : j - 4, uc = un >= 14 ? un - 14 : un;
        ro = xcd * 2 + (un >= 14); ct = (uc >> 1) & 7; rg = (uc & 1) * 4 + (w & 3);
        const int g1 = ks * 3 + cg;
        if (w < 4) {
            const char* cgp = ((ks >> 1) ? Ci : Cr) + (long)rb * (128 * 4096) + (ks & 1) * 2048;
            b_load(smem, q, cgp, (const char*)ab + g1 * (16 * B_SL), (u64)(((cg == 0 ? TD0 : cg == 1 ? TD1 : TD2) + rbl * 0x11111111u) & 0x77777777u) << 32 |
                   (((cg == 0 ? TC0 : cg == 1 ? TC1 : TC2) + rbl * 0x11111111u) & 0x77777777u), ks, cg == 2,
                   wo + (duty ? ct : 0) * 128, duty, hs, lane, w, r8 * 2 + (c6 & 1));
        } else {
            const int cw = w - 4;
            asm volatile("" ::"s"(xB), "s"(Hr), "s"(Hi));
            hs.off = hs_off(lane, g1 * 256, HS_X + ((rb * 3 + cg) * 4 + cw) * 256, ro, uc & 15);
            unsigned v1 = 0;
            hs_rmw(hs, v1, w == 5 ? M_LO | M_HI | M_XW | M_XR | M_OW : M_XW | M_XR);
            STAMP(0, 7);
            auto post = [&]() {
                if (w == 5) *reinterpret_cast<u32x2*>(smem + L_KEY + lane * 8) = u32x2{hs.off, hs_take<0>(v1)};
            };
            const unsigned dbase = (unsigned)((g1 * 16 + r8 * 2) * B_SL + (c6 & 1) * (c6 < 4 ? 80 : 64) * 64);
            const unsigned sg = (ks >> 1) ? 0x80008000u : 0u;
            if (c6 < 4 && !(cw & 1)) ab_mma<3>(smem, dbase, sg, lane, cw >> 1, 0, ab, post);
            else ab_mma<2>(smem, dbase, sg, lane, cw >> 1, (cw & 1) * (c6 < 4 ? 3 : 2), ab, post);
            hs.snap = hs_take<63>(v1);
            hs_add(hs, 1ull << (36 + ks));
            const unsigned pxb = (unsigned)(rb * 3 + cg) * 131072u, ob0 = (unsigned)((rb * 128 + ks * 16) * 896 + 448 + min(cg * 192, 320)) * 2u;
            if (cg == 2) y_mma<4>(smem, lane, cw, px, pxb, ks, out, ob0, 0, hs);
            else y_mma<5>(smem, lane, cw, px, pxb, ks, out, ob0, cg ? -64 : 256, hs);
            duty = false;
        }
    } else {
        const int qi = j - 24;
#define HGM 0x3102
        const int hg = HGM >> (4 * ks) & 3, rt = (xcd >> 2) * 8 + qi;
#define XT4 0x5401
        int kst = qi + (XT4 >> (4 * hg) & 15); kst = kst >= 7 ? kst - 7 : kst;
        ro = xcd * 2 + 1; ct = 3 + (qi >> 1); rg = (qi & 1) * 4 + (w & 3); duty = w < 4;
        const int co = 6 + qi;
        WO wq;
        wq.g = wo + ct * 128;
        const unsigned hv = qkv_attn(smem, xA, wqkv, wo, ws, rt, hg, tid, w, hs, kst, ro, co,
                                     (u64)(((ks >> 1) ? Ci : Cr) + (long)((xcd >> 2) * 8 + qi) * (128 * 4096) + (ks & 1) * 2048), qi, wq);
        STAMP(0, 21);
        if (w == 0) {
            hs_rmw(hs, ka, 1ull << hg | M_OW, lane == hg);
            if (!hs_new(hs, hv, mr)) again = 1ull << hg;
        }
        if (w >= 4) {
#pragma unroll 1
            for (int t = WE; t < 14; ++t) wo_req(wq, smem + W_A + t * 4096 + (w & 3) * 1024);
            wait_vm<0>();
        }
        BARL();
        wb = W_A;
    }
    if (duty) {
        volatile LDS_AS unsigned* go = (volatile LDS_AS unsigned*)(smem + L_GO);
        if (w == 0) { if (j < 24) { hs_rmw(hs, ka, M_OW); __builtin_amdgcn_s_sleep(PD2); } hs_wait2(hs, M_OW, M_OW, 0u, ka, kb); *go = 1u; }
        else lds_wait<SL2>(L_GO);
        asm volatile("" ::: "memory");
        STAMP(1, 0);
        if (w == 0) hs_add(hs, M_OR);
        oproj(smem + wb, (u16*)(ws + WS_O), out, ro, rg, ct, lane, ka, kb);
        if (!NK) hs_keep(ka, kb);
        if (again) { hs_wait(hs, mr, mr, 0u); hs_add(hs, again); }
    }
    STAMP(1, 24);
}

extern "C" void kernel_launch(void* const* d_in, const int* in_sizes, int n_in, void* d_out, int out_size,
                              void* d_ws, size_t ws_size, hipStream_t stream) {
    kf<<<256, 512, 0, stream>>>((const u16*)d_in[0], (const u16*)d_in[1], (const char*)d_in[3], (const char*)d_in[6], (const char*)d_in[7],
                                (const char*)d_in[4], (const char*)d_in[5], (const char*)d_in[2], (u16*)d_out, (char*)d_ws);
}
```

```cpp
#define MODE 1
#define TW 2000
#include <hip/hip_runtime.h>
#include <hip/hip_bf16.h>

typedef unsigned short u16;
typedef __attribute__((ext_vector_type(8))) short bf16x8;
typedef __attribute__((ext_vector_type(4))) short s16x4;
typedef __attribute__((ext_vector_type(4))) float f32x4;
typedef __attribute__((ext_vector_type(2))) __bf16 bf2;
typedef __attribute__((ext_vector_type(2))) unsigned u32x2;
typedef __attribute__((ext_vector_type(4))) unsigned u32x4;

#define LDS_AS __attribute__((address_space(3)))
#define GLB_AS __attribute__((address_space(1)))

constexpr int D = 448;

#define STAMP(kern, idx)
#define STAMPC(kern, idx)

typedef __attribute__((ext_vector_type(2))) float f32x2;
__device__ __forceinline__ unsigned pk2(float a, float b) {
    f32x2 v = {a, b};
    return __builtin_bit_cast(unsigned, __builtin_convertvector(v, bf2));
}
__device__ __forceinline__ void glds16a(const void* sbase, unsigned voff, const char* l) {
    const unsigned la = (unsigned)(size_t)(LDS_AS const char*)l;
    asm volatile("s_mov_b32 m0, %2\n\ts_nop 0\n\tglobal_load_lds_dwordx4 %0, %1" ::"v"(voff), "s"(sbase), "s"(la) : "memory");
}
__device__ __forceinline__ float vmax(float a, float b) { return __builtin_elementwise_maximum(a, b); }
__device__ __forceinline__ float vmax3(float a, float b, float c) { return vmax(vmax(a, b), c); }
__device__ __forceinline__ bf16x8 lds_rd128(const char* p) { return *reinterpret_cast<const bf16x8*>(p); }
__device__ __forceinline__ s16x4 lds_tr(const char* p) {
    return __builtin_amdgcn_ds_read_tr16_b64_v4i16((LDS_AS s16x4*)p);
}
__device__ __forceinline__ bf16x8 cat(s16x4 a, s16x4 b) {
    bf16x8 r;
    r[0] = a[0]; r[1] = a[1]; r[2] = a[2]; r[3] = a[3];
    r[4] = b[0]; r[5] = b[1]; r[6] = b[2]; r[7] = b[3];
    return r;
}
#define MFMA(a, b, c) __builtin_amdgcn_mfma_f32_16x16x32_bf16((a), (b), (c), 0, 0, 0)
template <int N> __device__ __forceinline__ void wait_vm() { asm volatile("s_waitcnt vmcnt(%0)" ::"i"(N) : "memory"); }
#define SB() __builtin_amdgcn_sched_barrier(0)
template <int NP, int NM> __device__ __forceinline__ void ilv() {
#pragma unroll
    for (int i = 0; i < NP; ++i) {
        __builtin_amdgcn_sched_group_barrier(0x8, 1, 0);
        __builtin_amdgcn_sched_group_barrier(0x100, 1, 0);
    }
    if constexpr (NM > 0) __builtin_amdgcn_sched_group_barrier(0x8, NM, 0);
}
#define BAR()                                               \
    do {                                                    \
        asm volatile("s_waitcnt lgkmcnt(0)" ::: "memory");  \
        __builtin_amdgcn_s_barrier();                       \
        asm volatile("" ::: "memory");                      \
    } while (0)
#define BARL()                                              \
    do {                                                    \
        asm volatile("" ::: "memory");                      \
        __builtin_amdgcn_s_barrier();                       \
        asm volatile("" ::: "memory");                      \
    } while (0)

#define BARK() do { __builtin_amdgcn_s_waitcnt(0xc07f); asm volatile("" ::: "memory"); __builtin_amdgcn_s_barrier(); asm volatile("" ::: "memory"); SB(); } while (0)
__device__ __forceinline__ int kperm(int r) { return (r & ~12) | ((r & 4) << 1) | ((r & 8) >> 1); }

constexpr int LDS_TOTAL = 163840;
constexpr int WS_O = 4 << 20, WS_PX = 8 << 20, WS_HS = 16 << 20;
#define CODEMAX 42880u
constexpr unsigned CODE_MAX = CODEMAX;

typedef unsigned long long u64;
struct Hs {
    const unsigned* base;
    unsigned off, snap;
};
constexpr int HS_O = 16 * 256, HS_X = 32 * 256;
constexpr u64 M_LO = 0xffffull, M_HI = 0xffff0000ull, M_OW = 0xfull << 48, M_OR = 1ull << 52, M_R14 = 0x3fffull << 16;
constexpr u64 M_XW = 0xfull << 32, M_XR = 0xf0ull << 32;
__device__ __forceinline__ unsigned hs_off(int lane, int b, int bx, int ro, int co) {
    const int bo = HS_O + ro * 256 - 48 * 4;
    return (unsigned)(lane * 4 + (lane < 32 ? b : lane < 48 ? bx - 32 * 4 : lane == 52 ? bo + 48 + co * 4 : bo));
}
template <int N> __device__ __forceinline__ unsigned hs_take(unsigned v) {
    unsigned r;
    asm volatile("s_waitcnt vmcnt(%2)\n\tv_mov_b32 %0, %1" : "=v"(r) : "v"(v), "i"(N) : "memory");
    return r;
}
__device__ __forceinline__ void hs_rmw(const Hs& h, unsigned& v, u64 m, unsigned d = 0u) {
    asm volatile("s_mov_b64 exec, %4\n\tglobal_atomic_add %0, %1, %2, %3 sc0 sc1\n\ts_mov_b64 exec, -1" : "+v"(v) : "v"(h.off), "v"(d), "s"(h.base), "s"(m) : "memory");
}
__device__ __forceinline__ void hs_add(const Hs& h, u64 m) {
    const unsigned one = 1u;
    asm volatile("s_mov_b64 exec, %3\n\tglobal_atomic_add %0, %1, %2 sc1\n\ts_mov_b64 exec, -1" ::"v"(h.off), "v"(one), "s"(h.base), "s"(m) : "memory");
}
__device__ __forceinline__ bool hs_new(const Hs& h, unsigned v, u64 m) { return (__ballot(v == h.snap) & m) == 0; }
#define PSL 2
__device__ __forceinline__ void hs_wait(const Hs& h, u64 m, u64 e, unsigned d) {
#pragma unroll 1
    for (unsigned spin = 0; spin < 40000u; ++spin) {
        unsigned v = 0;
        hs_rmw(h, v, e, d);
        if (hs_new(h, hs_take<0>(v), m)) return;
        __builtin_amdgcn_s_sleep(PSL);
    }
}
#define PD2 5
__device__ __forceinline__ void hs_wait2(const Hs& h, u64 m, u64 e, unsigned d, unsigned& a, unsigned& b) {
    unsigned n = 40000u;
    asm volatile("s_mov_b64 exec, %8\n"
                 "1:\n\tglobal_atomic_add %1, %3, %4, %5 sc0 sc1\n\ts_waitcnt vmcnt(1)\n\tv_cmp_eq_u32 vcc, %0, %6\n\ts_and_b64 vcc, vcc, %7\n\ts_cbranch_scc0 2f\n\t"
                 "global_atomic_add %0, %3, %4, %5 sc0 sc1\n\ts_waitcnt vmcnt(1)\n\tv_cmp_eq_u32 vcc, %1, %6\n\ts_and_b64 vcc, vcc, %7\n\ts_cbranch_scc0 2f\n\t"
                 "s_sub_u32 %2, %2, 1\n\ts_cbranch_scc0 1b\n"
                 "2:\n\ts_mov_b64 exec, -1"
                 : "+&v"(a), "+&v"(b), "+&s"(n) : "v"(h.off), "v"(d), "s"(h.base), "v"(h.snap), "s"(m), "s"(e) : "memory", "vcc", "scc");
}
template <int POL> __device__ __forceinline__ void st16_out(__amdgpu_buffer_rsrc_t r, unsigned off, u32x4 v) {
    __builtin_amdgcn_raw_buffer_store_b128(v, r, off, 0, POL);
}
__device__ __forceinline__ void st16_sc1(__amdgpu_buffer_rsrc_t r, unsigned off, u32x4 v) {
    __builtin_amdgcn_raw_buffer_store_b128(v, r, off, 0, 16);
}
__device__ __forceinline__ void glds16m(const void* sbase, unsigned voff, const char* l, u64 mask) {
    const unsigned la = (unsigned)(size_t)(LDS_AS const char*)l;
    asm volatile("s_mov_b32 m0, %2\n\ts_mov_b64 exec, %3\n\tglobal_load_lds_dwordx4 %0, %1\n\ts_mov_b64 exec, -1" ::"v"(voff), "s"(sbase), "s"(la), "s"(mask) : "memory");
}
__device__ __forceinline__ void glds16c(const void* sbase, unsigned voff, const char* l) {
    const unsigned la = (unsigned)(size_t)(LDS_AS const char*)l;
    asm volatile("s_mov_b32 m0, %2\n\ts_nop 0\n\tglobal_load_lds_dwordx4 %0, %1 sc1" ::"v"(voff), "s"(sbase), "s"(la) : "memory");
}
__device__ __forceinline__ void glds16mc(const void* sbase, unsigned voff, const char* l, u64 mask) {
    const unsigned la = (unsigned)(size_t)(LDS_AS const char*)l;
    asm volatile("s_mov_b32 m0, %2\n\ts_mov_b64 exec, %3\n\tglobal_load_lds_dwordx4 %0, %1 sc1\n\ts_mov_b64 exec, -1" ::"v"(voff), "s"(sbase), "s"(la), "s"(mask) : "memory");
}
template <int SL> __device__ __forceinline__ void lds_wait(unsigned a) {
    unsigned n = 1000000u, v;
    asm volatile("1:\n\ts_sleep %3\n\tds_read_b32 %0, %2\n\ts_sub_u32 %1, %1, 1\n\ts_cbranch_scc1 2f\n\ts_waitcnt lgkmcnt(0)\n\tv_cmp_eq_u32 vcc, 0, %0\n\ts_cbranch_vccnz 1b\n"
                 "2:\n\ts_waitcnt lgkmcnt(0)" : "=&v"(v), "+s"(n) : "v"(a), "i"(SL) : "memory", "vcc", "scc");
}

constexpr int A_SL = 16384, B_R0 = 6 * A_SL, B_SL = 20480, X_SL = 8192, H_SL = 10240, H_0 = 11 * X_SL, L_KEY = H_0 + 7 * H_SL, L_GO = 163328, W_0 = A_SL;
constexpr int CODE_PART = (CODE_MAX - 6144) / 7 / 128 * 128;
#define TT(i)
struct WO { const char* g; u64 pat; unsigned off, hi; };
__device__ __forceinline__ void wo_init(WO& q, int lane, int w) {
    q.g += w * (8 * 896);
    q.pat = 0ull;
#pragma unroll
    for (int i = 0; i < 21; ++i) q.pat |= (u64)(i % 7 == 6 ? 7 : (i % 7 + 1) / 2) << (3 * i);
    q.pat >>= 6 * w;
    q.hi = (unsigned)(lane >> 5) * (3 * 896);
    q.off = (unsigned)(((lane >> 5) * 16 + ((lane >> 3) & 3)) * 896 + ((lane & 7) ^ ((w & 1) * 4 + ((lane >> 4) & 1) * 2)) * 16);
    asm volatile("" : "+v"(q.off), "+v"(q.hi));
}
__device__ __forceinline__ void wo_req(WO& q, const char* l) {
    glds16a(q.g - ((unsigned)q.pat & 3) * (4 * 896), q.off + ((unsigned)q.pat & 4) * q.hi, l);
    q.g += 32 * 896; q.pat >>= 3;
}
#define SL1 1
#define SL2 1
#define N4 3
#define N5 4
#define N6 5
#define KA 3
#define V1 1
#define PDG 1
#define DEF 2
#define TC0 0x75643210u
#define TC1 0x67540132u
#define TD0 0x76543210u
#define TD1 TD0
#define TD2 TD0
#define TQ 0x76543210u
#define TC2 0x54763201u
#define LP 2
#define WP1 3
#define WP2 10
#define WE 2
struct BL {
    const char *xg, *hg;
    unsigned offX, offH[3];
    int kc;
};
__device__ __forceinline__ void reqX(BL& q, char* smem, int w, int sx) {
    const char* gx = q.xg + q.kc * 128;
    const char* la = smem + sx * X_SL + w * 2048;
    glds16a(gx, q.offX, la);
    glds16a(gx + 8 * 896, q.offX, la + 1024);
    q.kc = q.kc == 6 ? 0 : q.kc + 1;
}
__device__ __forceinline__ void reqH(BL& q, char* smem, int w, int k) {
    const char* gh = q.hg + q.kc * 128;
    const char* lh = smem + H_0 + k * H_SL + w * 2560;
    glds16a(gh, q.offH[0], lh);
    glds16a(gh, q.offH[1], lh + 1024);
    glds16m(gh, q.offH[2], lh + 2048, 0xffffffffull);
}
__device__ __forceinline__ void b_off(BL& q, int hn, int lane, int w) {
    const int r8 = lane >> 3, c = (lane & 7) ^ r8;
    q.offX = (unsigned)((w * 16 + r8) * 896 + c * 16);
#pragma unroll
    for (int i = 0; i < 3; ++i) q.offH[i] = (unsigned)(min(w * 20 + i * 8 + r8, hn) * 896 + (c ^ ((w & 1) * 4)) * 16);
}
__device__ __forceinline__ void b_load(char* smem, BL& q, const char* cg, const char* abg, u64 tb, int ks,
                                       bool narrow, const char* wog, bool duty, Hs& hs, int lane, int w, int pm) {
    const int r8 = lane >> 3, c = (lane & 7) ^ r8;
    unsigned offA[4];
#pragma unroll
    for (int i = 0; i < 4; ++i)
        offA[i] = (unsigned)(((w >> 1) * 64 + (((w & 1) * 2 + (i >> 1) + ks) & 3) * 16 + (i & 1) * 8 + r8) * 4096 + c * 16);
    const int rb0 = w * (narrow ? 32 : 40);
    unsigned offB = (unsigned)((c >> 2) * 10240 + (rb0 + r8) * 64 + (c & 3) * 16);
    u64 mB = narrow ? 1ull : ~0ull;
    int sa = 3;
    u64 tc = tb | 0x8888888800000000ull;
    const unsigned t3 = (unsigned)tb;
    auto reqC = [&]() {
        const char* g = cg + ((unsigned)tc & 15) * 128;
        const char* la = smem + sa * A_SL + w * 4096;
#pragma unroll
        for (int i = 0; i < 4; ++i) glds16a(g, offA[i], la + i * 1024);
        sa = sa == 5 ? 0 : sa + 1; tc >>= 4;
    };
    int sb = 0, lb0 = B_R0 + rb0 * 128;
    tb = tb << 1 | 0x1111111100000000ull;
    auto reqB = [&]() {
        const char* g = abg + ((unsigned)tb & 15) * B_SL;
        const char* lb = smem + lb0 + sb * B_SL;
#pragma unroll
        for (int i = 0; i < 4; ++i) glds16c(g + i * 512, offB, lb + i * 1024);
        glds16mc(g + 2048, offB, lb + 4096, mB);
        sb = sb == 2 ? 0 : sb + 1; tb >>= 4;
    };
    auto step = [&]() { reqB(); reqC(); wait_vm<13>(); BARL(); };
    u64 tt[11] = {0, 0, 0, 0, 0, 0, 0, 0, 0, 0, 0};
#pragma unroll 1
    for (int k = 2; k < 7; ++k) {
        reqH(q, smem, w, k); reqX(q, smem, w, k == 6 ? 10 : k);
        wait_vm<10>();
        BARL();
    }
    q.xg += 512 * 896;
    auto reqX2 = [&](int a, int b) {
#pragma unroll
        for (int i = a; i < b; ++i) reqX(q, smem, w, i == 6 ? 10 : i);
    };
    reqX2(0, N4);
    wait_vm<5 + 2 * N4>();
    BARL();
    reqX2(N4, N5);
    wait_vm<2 * N5>();
    BARL();
    TT(0);
    reqX2(N5, N6);
    unsigned own = (lane & 15) == pm, hv = 0, hw = 0;
    u64 m3 = 3u << (t3 << 1 & 14) | 3u << (t3 >> 3 & 14) | 3u << (t3 >> 7 & 14);
    asm volatile("" : "+v"(offA[0]), "+v"(offA[1]), "+v"(offA[2]), "+v"(offA[3]), "+v"(offB), "+v"(own));
    asm volatile("" : "+s"(mB), "+s"(m3), "+s"(abg), "+s"(cg), "+s"(lb0), "+s"(tb), "+s"(tc));
    if (w == 0) {
        const u32x2 t = *reinterpret_cast<const u32x2*>(smem + L_KEY + lane * 8);
        hs.off = t[0]; hs.snap = t[1];
    }
    wait_vm<2 * (N6 - KA)>();
    BARL();
    TT(1);
    reqX2(N6, 7);
    reqC();
    if (V1) reqC();
    wait_vm<4 + 4 * V1>();
    BARL();
    TT(2);
    if (w == 0) hs_add(hs, 1ull << pm);
    if (!V1) reqC();
#define PDA 5
    if (PDA && w == 0) { __builtin_amdgcn_s_sleep(PDA); hs_rmw(hs, hv, M_LO); }
    BARL();
    TT(3);
    if (w == 0) {
        if (!PDA) hs_rmw(hs, hv, M_LO);
        __builtin_amdgcn_s_sleep(PDG);
        hs_wait2(hs, m3, M_LO, own, hv, hw);
        *(volatile LDS_AS unsigned*)(smem + L_GO + 4) = 1u;
    } else lds_wait<SL1>(L_GO + 4);
    TT(4);
    reqB(); reqB();
    unsigned h2 = 0;
    if (w == 0) hs_rmw(hs, h2, M_LO);
    reqC();
    wait_vm<9>();
    asm volatile("" ::"v"(hv), "v"(hw));
    BARL();
    reqB(); reqC();
    {
        const unsigned v = hs_take<13>(h2);
        if (w == 0) { if (!hs_new(hs, v, M_LO)) hs_wait(hs, M_LO, M_LO, own); }
    }
    BARL();
    if (w == 0) hs_add(hs, 0x10001ull << pm | (duty ? M_OR : 0ull));
#pragma unroll 1
    for (int i = 1; i < 4; ++i) step();
    if (w == 0) hs_rmw(hs, h2, M_LO | M_HI, own);
    step();
    if (w == 0) { if (!hs_new(hs, hs_take<9>(h2), M_HI)) hs_wait(hs, M_HI, M_HI, 0u); }
#pragma unroll 1
    for (int i = 5; i < 13; ++i) step();
    WO wq;
    wq.g = wog;
    wo_init(wq, lane, w);
    reqB();
    if (duty) {
#pragma unroll
        for (int n = 0; n < 14; ++n) {
            if (n == WP1) { wait_vm<9 + WP1>(); BARL(); }
            if (n == WP2) { wait_vm<WP2>(); BARL(); }
            wo_req(wq, smem + W_0 + n * 4096 + w * 1024);
        }
    } else {
        wait_vm<9>(); BARL();
        wait_vm<0>(); BARL();
    }
    wait_vm<0>();
    BARL();
}
template <int NE, class F>
__device__ __forceinline__ void ab_mma(char* smem, unsigned dbase, unsigned sg, int lane, int sh, int ef, u16* ab, F post) {
    const int fr = lane & 15, fq = lane >> 4;
    const int z0 = fr * 128 + ((fq ^ (fr & 7)) * 16), z1 = z0 ^ 64;
    unsigned a0 = sh * 4096 + z0, a1 = sh * 4096 + z1, b0 = H_0 + ef * 2048 + z0, b1 = H_0 + ef * 2048 + z1;
    unsigned doff = dbase + (unsigned)(sh * 10240 + (ef * 16 + fr) * 64 + ((fq & 1) * 2 + (fq >> 1)) * 16);
    asm volatile("" : "+v"(a0), "+v"(a1), "+v"(b0), "+v"(b1), "+v"(doff));
    const char *sa0 = smem + a0, *sa1 = smem + a1, *sb0 = smem + b0, *sb1 = smem + b1;
    const __amdgpu_buffer_rsrc_t rs = __builtin_amdgcn_make_buffer_rsrc(ab, 0, 192 * B_SL, 0x00020000);
    f32x4 acc[NE][2];
    bf16x8 xf[2][2][2], hf[2][2][NE];
    auto rd = [&](int k, int i) {
        const int h = i >= 2 + NE, n = i - h * (2 + NE);
        if (n < 2) xf[k & 1][h][n] = lds_rd128((h ? sa1 : sa0) + (k == 6 ? 10 : k) * X_SL + n * 2048);
        else hf[k & 1][h][n - 2] = lds_rd128((h ? sb1 : sb0) + k * H_SL + (n - 2) * 2048);
    };
#pragma unroll 1
    for (int p = 0; p < 2; ++p) {
#pragma unroll
        for (int a = 0; a < NE; ++a) {
            acc[a][0] = f32x4{0.f, 0.f, 0.f, 0.f}; acc[a][1] = f32x4{0.f, 0.f, 0.f, 0.f};
            asm volatile("" : "+v"(acc[a][0]), "+v"(acc[a][1]));
        }
#pragma unroll
        for (int k = 0; k < 8; ++k) {
            if (k == 0) BARL();
            else if (k == KA) { if (p) wait_vm<0>(); BARL(); }
            else if (k == 7) { if (p) BAR(); }
            else if (!p) { if (k == 6) { post(); BAR(); } else BARL(); }
            if (k == 0 && p == 0) STAMP(0, 1);
            SB();
#pragma unroll
            for (int i = 0; i < 4 * NE; ++i) {
                const int h = i >= 2 * NE, n = i - h * 2 * NE;
                if (k == 7) acc[n >> 1][n & 1] = MFMA(xf[0][h][n & 1], hf[0][h][n >> 1], acc[n >> 1][n & 1]);
                else if (k) asm volatile("v_mfma_f32_16x16x32_bf16 %0, %1, %2, %0" : "+v"(acc[n >> 1][n & 1]) : "v"(xf[~k & 1][h][n & 1]), "v"(hf[~k & 1][h][n >> 1]));
                SB();
                if (k < 7 && i < 4 + 2 * NE) rd(k, i);
                SB();
            }
        }
#pragma unroll
        for (int mi = 0; mi < NE; ++mi) {
            const f32x4 a = acc[mi][0], b = acc[mi][1];
            auto x = __builtin_amdgcn_permlane16_swap(pk2(a[0], a[1]) ^ sg, pk2(b[0], b[1]) ^ sg, false, false);
            auto y = __builtin_amdgcn_permlane16_swap(pk2(a[2], a[3]) ^ sg, pk2(b[2], b[3]) ^ sg, false, false);
            st16_sc1(rs, doff + mi * 1024u, u32x4{x[0], y[0], x[1], y[1]});
        }
        SB();
        doff += B_SL;
    }
}
template <int NF>
__device__ __forceinline__ void y_mma(char* smem, int lane, int cw, u16* px, unsigned pxb, int ks, u16* out, unsigned ob0, Hs& hs) {
    const int fr = lane & 15, fq = lane >> 4;
    const int mh = cw >> 1, nh = cw & 1;
    f32x4 acc[5][4];
#pragma unroll
    for (int a = 0; a < NF; ++a)
#pragma unroll
        for (int b = 0; b < 4; ++b) { acc[a][b] = f32x4{0.f, 0.f, 0.f, 0.f}; asm volatile("" : "+v"(acc[a][b])); }
    const int z0 = fr * 128 + ((fq ^ (fr & 7)) * 16), z1 = z0 ^ 64;
    unsigned a0 = 3 * A_SL + mh * 8192 + z0, a1 = 3 * A_SL + mh * 8192 + z1, b0 = B_R0 + nh * (NF * 2048) + z0, b1 = B_R0 + nh * (NF * 2048) + z1;
    unsigned pl = pxb + (unsigned)(cw * 8192 + lane * 16), ph = pl + (unsigned)(6144 - 512 - lane * 8);
    if (NF == 5) asm volatile("" : "+v"(ph));
    unsigned ob = ob0 + (unsigned)((mh * 64 + fr) * 896 + nh * (NF * 16) + (fq & 1) * 16 + (fq >> 1) * 8) * 2u;
    const unsigned one = fq == (fr >> 2) ? 0x3f80u << ((fr & 1) * 16) : 0u, d0 = (fr & 2) ? 0u : one, d1 = (fr & 2) ? one : 0u;
    u32x4 s0 = {d0, d1, 0u, 0u}, s1 = {0u, 0u, d0, d1};
    unsigned own = lane == 32 + ks;
    asm volatile("" : "+v"(a0), "+v"(a1), "+v"(b0), "+v"(b1), "+v"(pl), "+v"(ob), "+v"(s0), "+v"(s1), "+v"(own));
    const char *sa0 = smem + a0, *sa1 = smem + a1, *sb0 = smem + b0, *sb1 = smem + b1;
    const __amdgpu_buffer_rsrc_t rs = __builtin_amdgcn_make_buffer_rsrc(px, 0, 48 * 4 * 32768, 0x00020000);
    const __amdgpu_buffer_rsrc_t rs_out = __builtin_amdgcn_make_buffer_rsrc(out, 0, 2048 * 896 * 2, 0x00020000);
    bf16x8 cf[2][4], xf[2][NF];
    auto rdx = [&](int mi, int k, int st) { xf[k][mi] = lds_rd128((k ? sb1 : sb0) + st * B_SL + mi * 2048); };
    auto rdc = [&](int l, int k, int st) { cf[k][l] = lds_rd128((k ? sa1 : sa0) + st * A_SL + l * 2048); };
    auto mf = [&](int mi, int l, int k) {
        asm volatile("v_mfma_f32_16x16x32_bf16 %0, %1, %2, %0" : "+v"(acc[mi][l]) : "v"(cf[k][l]), "v"(xf[k][mi]));
        SB();
    };
    int da = -3 * A_SL;
    auto head = [&](int st, int lo) {
        int n = 0;
        __builtin_amdgcn_s_waitcnt(0xc47f);
        SB();
#pragma unroll
        for (int i = 4 * lo; i < 16; ++i) {
            mf(((i >> 1) ^ (i >> 2)) & 1, i >> 2, (i ^ (i >> 1)) & 1);
            if (n < 2 * NF - 4) rdx(2 + (n >> 1), n & 1, st);
            if (st == 2 && n == 8) sa0 += da;
            if (st == 2 && n == 10) { sa1 += da; da = -da; }
            ++n;
            SB();
        }
        if (NF == 5) {
#pragma unroll
            for (int i = 0; i < 8 - 2 * lo; ++i) mf(2, 3 - (i >> 1), (i ^ (i >> 1)) & 1);
        }
    };
    auto rest = [&](int st, int lo) {
#pragma unroll
        for (int i = 4 * lo; i < 16; ++i) {
            mf(NF - 2 + (((i >> 1) ^ (i >> 2)) & 1), i >> 2, (i ^ (i >> 1)) & 1);
            if ((i & 3) >= 2) rdc(i >> 2, ~i & 1, st);
            else if (i - 4 * lo < 8) rdx((i >> 2) - lo, i & 1, st);
            else if (lo) rdc(0, i & 1, st);
            SB();
        }
    };
    STAMP(0, 4);
    BARL();
    STAMP(0, 5); STAMPC(0, 3);
#pragma unroll
    for (int i = 0; i < 16; ++i) {
        if ((i & 3) >= 2) rdc(i >> 2, i & 1, 0);
        else if (i < 8) rdx(i >> 2, i & 1, 0);
        SB();
    }
#pragma unroll 1
    for (int t = 0; t < 4; ++t) {
        head(0, 0);
        wait_vm<0>();
        BARK();
        rest(1, 0); head(1, 0);
        BARK();
        rest(2, 0); head(2, 0);
        BARK();
        rest(0, 0);
    }
    bf16x8 kf[4];
    unsigned ka = 0, kb = 0;
    head(0, 0); BARK();
    rest(1, 0); head(1, 1); BARK();
    kf[0] = cf[0][0]; kf[1] = cf[1][0];
    rest(2, 1); head(2, 1); BARK();
    hs_rmw(hs, kb, M_XR);
    kf[2] = cf[0][0]; kf[3] = cf[1][0];
    rest(0, 1);
    STAMP(0, 18); STAMPC(0, 4);
    const int pw = ks * 32768;
    auto put = [&](int p, int h) {
        const int sl = 2 * (p - 1) + h;
        const f32x4 a = acc[h < 2 ? 2 * h : NF - 1][p], b = acc[h < 2 ? 2 * h + 1 : 0][p];
        if (h < 2) __builtin_amdgcn_raw_buffer_store_b128(u32x4{pk2(a[0], a[1]), pk2(a[2], a[3]), pk2(b[0], b[1]), pk2(b[2], b[3])}, rs, pl + (sl & 3) * 1024u, pw + (sl >> 2 << 12), 16);
        else __builtin_amdgcn_raw_buffer_store_b64(u32x2{pk2(a[0], a[1]), pk2(a[2], a[3])}, rs, ph + p * 512u, pw, 16);
    };
    constexpr int NG = 2 * NF - 4, NL = 12 + 3 * NG;
#pragma unroll
    for (int i = 0; i < NL + 2 * NF; ++i) {
        const int j = i - 12, l = i < 12 ? 1 + (i >> 2) : i < NL ? 1 + j / NG : 0, mi = i < 12 ? (i >> 1) & 1 : i < NL ? 2 + j % NG / 2 : (i - NL) >> 1;
        acc[mi][l] = MFMA(cf[i & 1][l], xf[i & 1][mi], acc[mi][l]);
        SB();
        if (i < NG) rdx(2 + (i >> 1), i & 1, 0);
        const int e = i - 2;
        if (e == 4 || e == 8 || e == 12) put(e >> 2, 0);
        if (e > 12 && e <= NL && (e - 12) % NG == 4 % NG) put((e - 13) / NG + 1, 1);
        if (NF == 5 && e > 12 && e <= NL && (e - 12) % NG == 0) put((e - 12) / NG, 2);
        if (i > NL + 1) rdx((i - NL - 2) >> 1, i & 1, 1);
        if (i == NL + 3) BARL();
        SB();
    }
    rdx(NF - 1, 0, 1); rdx(NF - 1, 1, 1);
#pragma unroll
    for (int i = 0; i < 4 * NF; ++i) {
        const int mi = (i >> 1) % NF;
        acc[mi][0] = MFMA(kf[(i >= 2 * NF) * 2 + (i & 1)], xf[i & 1][mi], acc[mi][0]);
        SB();
        if (i < 2 * NF) rdx(mi, i & 1, 2);
        SB();
    }
#pragma unroll
    for (int mi = 0; mi < NF; ++mi) asm volatile("" : "+v"(acc[mi][0]));
    STAMP(0, 19);
    const bool all = hs_new(hs, hs_take<0>(kb), M_XR);
    STAMP(0, 20);
    hs_rmw(hs, ka, M_XW, own);
    int pr[4];
#pragma unroll
    for (int l = 1; l < 4; ++l) pr[l - 1] = ((ks - l) & 3) * 32768 + (l == 3 ? 4096 : 0);
    pr[3] = pr[2] - 4096;
    unsigned pl2 = pl, ph2 = ph;
    u32x4 s01 = s0 | s1;
    __amdgpu_buffer_rsrc_t ro2 = rs_out;
    asm volatile("" : "+s"(pr[0]), "+s"(pr[1]), "+s"(pr[2]), "+s"(pr[3]), "+s"(ro2));
    asm volatile("" : "+v"(pl2), "+v"(ph2), "+v"(s01));
    auto fin = [&](int pr) {
        const f32x4 a = acc[2 * pr][0], b = acc[pr < 2 ? 2 * pr + 1 : 2 * pr][0];
        auto x = __builtin_amdgcn_permlane16_swap(pk2(a[0], a[1]), pk2(b[0], b[1]), false, false);
        auto y = __builtin_amdgcn_permlane16_swap(pk2(a[2], a[3]), pk2(b[2], b[3]), false, false);
        if (pr < 2 || !(fq & 1)) st16_out<0>(ro2, ob + pr * 64u, u32x4{x[0], y[0], x[1], y[1]});
        SB();
    };
    u32x2 o5 = {0u, 0u};
    {
        const f32x4 z = {0.f, 0.f, 0.f, 0.f};
#pragma unroll
        for (int h = 0; h < 2; ++h) {
            const f32x4 a = acc[2 * h][0], b = acc[2 * h + 1][0];
            const bf16x8 p = __builtin_bit_cast(bf16x8, u32x4{pk2(a[0], a[1]), pk2(a[2], a[3]), pk2(b[0], b[1]), pk2(b[2], b[3])});
            acc[2 * h][0] = MFMA(p, __builtin_bit_cast(bf16x8, s0), z);
            acc[2 * h + 1][0] = MFMA(p, __builtin_bit_cast(bf16x8, s1), z);
            asm volatile("" : "+v"(acc[2 * h][0]), "+v"(acc[2 * h + 1][0]));
        }
        if (NF == 5) { const f32x4 a = acc[4][0]; o5 = u32x2{pk2(a[0], a[1]), pk2(a[2], a[3])}; acc[4][0] = z; asm volatile("" : "+v"(o5), "+v"(acc[4][0])); }
    }
    __builtin_amdgcn_s_sleep(PD2 - 2);
    hs_wait2(hs, M_XW ^ 1ull << (32 + ks), M_XW ^ 1ull << (32 + ks), 0u, ka, kb);
    STAMP(1, 0);
    bf16x8 pv[2][3];
    u32x2 p5[3];
#pragma unroll
    for (int h = 0; h < (NF + 1) / 2; ++h)
#pragma unroll
        for (int l = 1; l < 4; ++l) {
            const int sl = 2 * (l - 1) + h;
            if (h < 2) pv[h][l - 1] = __builtin_bit_cast(bf16x8, __builtin_amdgcn_raw_buffer_load_b128(rs, pl2 + (sl & 3) * 1024u, pr[l - 1], 16));
            else p5[l - 1] = __builtin_amdgcn_raw_buffer_load_b64(rs, ph2 + l * 512u, pr[l == 3 ? 3 : l - 1], 16);
            SB();
        }
    if (!all) hs_add(hs, 1ull << (32 + ks));
#pragma unroll
    for (int h = 0; h < (NF + 1) / 2; ++h) {
        if (h < 2) {
#pragma unroll
            for (int l = 0; l < 3; ++l) {
                acc[2 * h][0] = MFMA(pv[h][l], __builtin_bit_cast(bf16x8, s0), acc[2 * h][0]);
                acc[2 * h + 1][0] = MFMA(pv[h][l], __builtin_bit_cast(bf16x8, s1), acc[2 * h + 1][0]);
                SB();
                if (h == 1 && l == 1 && NF == 5) fin(0);
            }
        } else {
            acc[4][0] = MFMA(__builtin_bit_cast(bf16x8, (u32x4{p5[0][0], p5[0][1], p5[1][0], p5[1][1]})), __builtin_bit_cast(bf16x8, s01), acc[4][0]);
            acc[4][0] = MFMA(__builtin_bit_cast(bf16x8, (u32x4{p5[2][0], p5[2][1], o5[0], o5[1]})), __builtin_bit_cast(bf16x8, s01), acc[4][0]);
            SB();
        }
    }
    STAMP(1, 1);
    asm volatile("" ::"v"(ka), "v"(kb), "v"(acc[NF - 1][0]));
    if (NF == 4) {
        u32x4 v[2], p0, p1;
#pragma unroll
        for (int pr = 0; pr < 2; ++pr) {
            const f32x4 a = acc[2 * pr][0], b = acc[2 * pr + 1][0];
            auto x = __builtin_amdgcn_permlane16_swap(pk2(a[0], a[1]), pk2(b[0], b[1]), false, false);
            auto y = __builtin_amdgcn_permlane16_swap(pk2(a[2], a[3]), pk2(b[2], b[3]), false, false);
            v[pr] = u32x4{x[0], y[0], x[1], y[1]};
        }
#pragma unroll
        for (int i = 0; i < 4; ++i) {
            p0[i] = __builtin_amdgcn_update_dpp(v[0][i], v[1][i], 0x128, 0xf, 0xc, false);
            p1[i] = __builtin_amdgcn_update_dpp(v[1][i], v[0][i], 0x128, 0xf, 0x3, false);
        }
        const unsigned o2 = ob - (unsigned)(fr & 8) * (1792 - 8);
        st16_out<16>(ro2, o2, p0);
        st16_out<16>(ro2, o2 + 8 * 1792u, p1);
    } else
    fin(1);
    if (NF == 5) fin(2);
}

constexpr int Q_ASL = 18432, Q_WR = 3 * Q_ASL, Q_WSL = 21504, GP = 1216, Q_DUMMY = 162816, W_A = Q_DUMMY - 14 * 4096, Q_TAB = 84 * GP;
__device__ __forceinline__ void qkv_reqW(char* smem, const char* wg, unsigned off, int c, int kw, int sw) {
    const char* g = wg + (long)kw * (32 * 2688);
    const char* sb = smem + Q_WR + sw * Q_WSL + c * 1792;
#pragma unroll
    for (int k = 0; k < 6; ++k) glds16m(g + (k & 1) * (8 * 2688) + (k >> 1) * 896, off, sb + (k & 1) * 896 + (k >> 1) * 7168, 0xffffffffffffffull);
}
struct QX { unsigned off; int r0, r1, r2; };
__device__ __forceinline__ QX qkv_offX(int c, int lane, int s0) {
    QX q;
    q.off = (unsigned)((lane >> 3) * 1792 + ((lane & 7) ^ (lane >> 3)) * 16);
    q.r0 = max(s0 + (c < 2 ? c - 16 : 16 * c - 32), 0) * 896;
    q.r1 = (s0 + 16 * c + 32) * 896;
    q.r2 = q.r1 + (c < 2 ? 64 : -63) * 896;
    return q;
}
__device__ __forceinline__ void qkv_reqX(char* smem, const u16* __restrict__ xA, const QX& q, int c, int kx, int sx) {
    const char* g = (const char*)xA + kx * 128;
    const char* sb = smem + sx * Q_ASL + c * 1024;
    glds16a(g + q.r0, q.off, sb);
    glds16a(g + q.r1, q.off, sb + 4096);
    glds16a(g + q.r2, q.off, sb + 2 * 4096);
    glds16a(g + q.r1 + 896, q.off, sb + 3 * 4096);
    if (c < 2) glds16a(g + q.r1 + 65 * 896, q.off, sb + 4 * 4096);
}
template <int V> struct QS {
    static constexpr int col(int i) { constexpr int t[3][6] = {{0, 1, 2, 5, 3, 4}, {2, 3, 1, 0, 9, 10}, {0, 1, 2, 2, 3, 4}}; return t[V][i]; }
    static constexpr int wof(int i) { return V == 1 ? ((col(i) + 5) / 7) * 7168 + ((col(i) + 5) % 7 - 5) * 32 : col(i) * 32; }
    static constexpr int lo(int i) { return V == 0 ? (i == 3 ? 3 : 1) : V == 1 ? (i == 2 || i == 3 ? 1 : 0) : 0; }
    static constexpr int hi(int i) { return i != 3 || V == 0 ? 9 : V == 1 ? 3 : 0; }
};
#define RS2 0
#define LB 1
#define LC 3
#define LW 4
template <int V>
__device__ __forceinline__ void qkv_mma(char* smem, int lane, int G0, u64 (&tt)[11]) {
    typedef QS<V> S;
    const int fr = lane & 15, fq = lane >> 4;
    const int u = fq & 1, ah = fq >> 1, q = (lane >> 2) & 3, p = lane & 3;
    constexpr int R0 = V == 0 ? 1 : 0;
    f32x4 acc[6][9];
    unsigned aw = Q_WR + Q_WSL + (16 * ah + 4 * u + q) * 224 + 8 * p + (V < 2 ? G0 * 32 : (G0 >> 3) * 7168 + 64);
    unsigned ax0 = fr * 128 + ((fq ^ (fr & 7)) * 16), ax1 = ax0 ^ 64;
    unsigned ai = (G0 * 4 + fq) * GP + fr * 8, ah0 = ai + (fr & 8) * 64;
#pragma unroll
    for (int i = 0; i < 6; ++i)
#pragma unroll
        for (int r = S::lo(i); r < S::hi(i); ++r) { acc[i][r] = f32x4{0.f, 0.f, 0.f, 0.f}; asm volatile("" : "+v"(acc[i][r])); }
    asm volatile("" : "+v"(aw), "+v"(ax0), "+v"(ax1), "+v"(ai));
    if (V) asm volatile("" : "+v"(ah0));
    const char *pw = smem + aw, *px0 = smem + ax0, *px1 = smem + ax1;
    bf16x8 xf[9];
    s16x4 wl[6], wh[6];
    auto rdw = [&](int i, int h) {
        if (S::hi(i) == 0) return;
        if (h) wh[i] = lds_tr(pw + S::wof(i) + 8 * 224); else wl[i] = lds_tr(pw + S::wof(i));
    };
    int cw = 0x2108, cx = 0x24, dw = 0, dx = 0;
    auto tick = [&](int kind, bool extra) {
#pragma unroll
        for (int g = 0; g < 3; ++g) {
            if (g == 2 && (kind == 1 || extra)) BARK();
            int n = 0;
#pragma unroll
            for (int r = 0; r < 9; ++r)
#pragma unroll
                for (int e = 0; e < 2; ++e) {
                    const int i = 2 * g + (e ^ (r & 1));
                    if (r < S::lo(i) || r >= S::hi(i)) { if (g == 2 && e == 1 && r >= R0) { xf[r] = lds_rd128((kind ? px0 : px1) + r * 2048); SB(); } continue; }
                    asm volatile("v_mfma_f32_16x16x32_bf16 %0, %1, %2, %0" : "+v"(acc[i][r]) : "v"(cat(wl[i], wh[i])), "v"(xf[r]));
                    SB();
                    if (g < 2 ? n < 4 : (n < 8 && !(n & 1))) { const int m = g < 2 ? n : n >> 1; rdw(2 * ((g + 1) % 3) + (m >> 1), m & 1); }
                    if (g == 2 && e == 1) xf[r] = lds_rd128((kind ? px0 : px1) + r * 2048);
                    if (g == 0) {
                        if (n == 4) { dw = cw & 1 ? -4 * Q_WSL : Q_WSL; asm volatile("" : "+s"(dw)); }
                        if (n == 6) { cw >>= 1; asm volatile("" : "+s"(cw)); }
                        if (n == 8 && kind) { dx = cx & 1 ? -2 * Q_ASL : Q_ASL; asm volatile("" : "+s"(dx)); }
                        if (n == 10 && kind) { cx >>= 1; asm volatile("" : "+s"(cx)); px0 += dx; }
                        if (n == 12 && kind) px1 += dx;
                    }
                    if (g == 1 && n == 5) pw += dw;
                    ++n;
                    SB();
                }
        }
    };
    auto last = [&]() {
        int fi[48], fw[48], nf = 0, na = 99;
        u32x2 cv[48];
        auto behind = [&](int f) {
            const int a = f - LW, b = f - LC;
            if (a >= 0 && a < nf) {
                const int r = fw[a];
                *reinterpret_cast<u32x2*>(smem + (r ? ai : ah0) + S::col(fi[a]) * 4 * GP + (r == 0 ? 0 : r < 5 ? 128 * r - 64 : 128 * r)) = cv[a];
                SB();
            }
            if (b >= 0 && b < nf) { const f32x4 d = acc[fi[b]][fw[b]]; cv[b] = u32x2{pk2(d[0], d[1]), pk2(d[2], d[3])}; asm volatile("" : "+v"(cv[b])); }
            if (a == na + LB) { SB(); BARK(); }
        };
#pragma unroll
        for (int h = 0; h < (RS2 ? 6 : 3); ++h) {
            const int g = h % 3;
            int n = 0;
            if (h == 3) na = nf - 1;
#pragma unroll
            for (int r = 0; r < 9; ++r)
#pragma unroll
                for (int e = 0; e < 2; ++e) {
                    const int i = 2 * g + e;
                    if (r < S::lo(i) || r >= S::hi(i) || (RS2 && (0x67 >> r & 1) != (h < 3))) continue;
                    asm volatile("v_mfma_f32_16x16x32_bf16 %0, %1, %2, %0" : "+v"(acc[i][r]) : "v"(cat(wl[i], wh[i])), "v"(xf[r]));
                    SB();
                    if (n < 2 && h < 2) { rdw(2 * h + 2 + n, 0); rdw(2 * h + 2 + n, 1); }
                    fi[nf] = i; fw[nf] = r; ++nf;
                    behind(nf - 1);
                    ++n;
                    SB();
                }
        }
        asm volatile("s_nop 7");
#pragma unroll
        for (int f = 0; f < LW; ++f) { behind(nf + f); SB(); }
    };
    TT(9);
    __builtin_amdgcn_s_setprio(LP);
    BARL();
    TT(0); TT(5);
#pragma unroll
    for (int r = R0; r < 9; ++r) {
        if (r - R0 < 4) { rdw((r - R0) >> 1, (r - R0) & 1); SB(); }
        xf[r] = lds_rd128(px0 + r * 2048);
        SB();
    }
#pragma unroll 1
    for (int s = 0; s < 6; ++s) { tick(0, s == 0); tick(1, false); }
    TT(1); TT(6);
    tick(0, false);
    BARK();
    TT(2);
    last();
    TT(3); TT(7);
}

struct AMk { f32x4 c0, c1; u32x4 m; };
__device__ __forceinline__ AMk attn_mask(unsigned ln, bool top) {
    AMk t;
    const int out = (int)(~((0x1feu << (ln & 15)) & (top ? ~0xffu : ~0u)) >> (4 * (ln >> 4)));
    unsigned b0[4], b1[4];
#pragma unroll
    for (int i = 0; i < 4; ++i) {
        b0[i] = (unsigned)__builtin_amdgcn_sbfe(out, i, 1); b1[i] = (unsigned)__builtin_amdgcn_sbfe(out, 16 + i, 1);
        t.c0[i] = __uint_as_float(b0[i] & 0xf149f2cau);
        t.c1[i] = __uint_as_float(b1[i] & 0xf149f2cau);
    }
#pragma unroll
    for (int i = 0; i < 2; ++i) {
        t.m[i] = ~((b0[2 * i] & 0xffffu) | (b0[2 * i + 1] & 0xffff0000u));
        t.m[2 + i] = ~((b1[2 * i] & 0xffffu) | (b1[2 * i + 1] & 0xffff0000u));
    }
    asm volatile("" : "+v"(t.c0), "+v"(t.c1), "+v"(t.m));
    return t;
}
struct AA { u32x4 a; u32x2 b; };
__device__ __forceinline__ AA attn_addr(unsigned ln, int w, int rt, int hg) {
    AA t;
    const unsigned fr = ln & 15, fq = ln >> 4, b = (w >> 1) * 16, z = (w & 1) * 576, gq = ((fq & 1) * 2 + (fq >> 1)) * GP + z, gv = (56 + (ln & 3)) * GP + z;
    t.a[0] = gq + (b + fr) * 8;
    t.a[1] = fq == 3 ? 1152 - 64 : t.a[0];
    t.a[2] = gq + (b + 16 + min(fr, 7u)) * 8;
    t.a[3] = gv + (b + (ln >> 2)) * 8;
    t.b[0] = gv + (b + 16 + min(ln >> 2, 7u)) * 8;
    t.b[1] = (unsigned)(rt * 56 + hg * 14 + fq) * 2048u + (w * 16 + fr) * 16u;
    asm volatile("" : "+v"(t.a), "+v"(t.b));
    return t;
}
#define Q5 tp(0)
#define Q5N 1
#define Q6 tp(1)
#define Q7 tp(2); tp(3)
#define Q8 tw()
#define QD 0
__device__ __forceinline__ unsigned qkv_attn(char* smem, const u16* xA, const u16* wqkv, const char* wo, char* ws, int rt, int hg, int tid, int w,
                                         Hs& hs, int kst, int ro, int co, u64 cb, int qi, WO& wq) {
    const int lane = tid & 63, s0 = rt * 128, c = w & 3;
    auto st = [&](int i) { const int k = kst + i; return k >= 7 ? k - 7 : k; };
    u16* o = (u16*)(ws + WS_O);
    AMk am;
    AA aa;
    unsigned hv2 = 0;
    u64 tt[11] = {0, 0, 0, 0, 0, 0, 0, 0, 0, 0, 0};
    if (w >= 4) {
        if (w == 4) qkv_mma<0>(smem, lane, 0, tt);
        else if (w == 5) qkv_mma<1>(smem, lane, 5, tt);
        else qkv_mma<2>(smem, lane, 7 * w - 33, tt);
        __builtin_amdgcn_s_setprio(0);
        const char* tb = smem + W_A + c * 1024 + lane * 16;
        aa.a = *reinterpret_cast<const u32x4*>(tb); aa.b = *reinterpret_cast<const u32x2*>(tb + 4096);
        tb = smem + Q_TAB + lane * 16;
        am.c0 = *reinterpret_cast<const f32x4*>(tb); am.c1 = *reinterpret_cast<const f32x4*>(tb + 1024);
        am.m = *reinterpret_cast<const u32x4*>(tb + 2048);
        BAR();
        TT(4);
    } else {
#define LP0 1
        __builtin_amdgcn_s_setprio(LP0);
        const char* wg = (const char*)wqkv + ((c >> 1) * 16 + (c & 1) * 4) * 2688 + hg * 224;
        const unsigned offW = (unsigned)(lane * 16 + (lane * 37 >> 9) * (2688 - 224));
        if (QD) __builtin_amdgcn_s_sleep(QD);
        TT(0);
#define XF 1
        if (!XF) { qkv_reqW(smem, wg, offW, c, 2 * kst, 1); SB(); }
        const QX qx = qkv_offX(c, lane, s0);
        qkv_reqX(smem, xA, qx, c, kst, 0);
        if (XF) { SB(); qkv_reqW(smem, wg, offW, c, 2 * kst, 1); }
        TT(1);
        qkv_reqW(smem, wg, offW, c, 2 * kst + 1, 2);
        TT(2);
        wait_vm<6>();
        TT(4);
        BARL();
        __builtin_amdgcn_s_setprio(0);
        STAMP(0, 1);
        unsigned hv = 0;
        if (w == 0) { hs.off = hs_off(lane, HS_O + rt * 256, 0, ro, co); hs_rmw(hs, hv, M_R14 | M_OW); }
#define XO 0
        const bool late = XO == 1 || (XO == 2 && ((hg + 1) & 2));
        if (!late) qkv_reqX(smem, xA, qx, c, st(1), 1);
        qkv_reqW(smem, wg, offW, c, 2 * st(1), 3);
        if (late) wait_vm<6>(); else wait_vm<10>();
        BARL();
        STAMP(0, 9);
        int sa = 4, sx = late ? 1 : 2;
        const unsigned tc = (unsigned)((c * 32 + (lane >> 1)) * 4096), ts = (lane & 1) * 4;
        const unsigned tb = (TQ + qi * 0x11111111u) & 0x77777777u | 0x88888888u;
        auto tp = [&](int i) { asm volatile("s_mov_b32 m0, %2\n\ts_nop 0\n\tglobal_load_lds_dword %0, %1" ::"v"(tc + ((tb >> 8 * i >> ts & 15) << 7)), "s"(cb), "i"(Q_DUMMY) : "memory"); };
#pragma unroll 1
        for (int s = 0; s < 5; ++s) {
            if (late) qkv_reqX(smem, xA, qx, c, st(s + 1), sx);
            qkv_reqW(smem, wg, offW, c, 2 * st(s + 1) + 1, sa);
            qkv_reqW(smem, wg, offW, c, 2 * st(s + 2), sa == 4 ? 0 : sa + 1);
            if (late) wait_vm<6>();
            else {
                qkv_reqX(smem, xA, qx, c, st(s + 2), sx);
                if (c < 2) wait_vm<11>(); else wait_vm<10>();
            }
            sa = sa >= 3 ? sa - 3 : sa + 2; sx = sx == 2 ? 0 : sx + 1;
            if (w == 0 && s == 3) { hs.snap = hs_take<63>(hv); hs_add(hs, M_OR); }
            BARL();
        }
        if (late) qkv_reqX(smem, xA, qx, c, st(6), 0);
        qkv_reqW(smem, wg, offW, c, 2 * st(6) + 1, 4);
        int l2 = lane;
        Q5;
        asm volatile("" : "+v"(l2));
        __builtin_assume(l2 >= 0 && l2 < 64);
        am = attn_mask(l2, rt + (w >> 1) == 0);
        aa = attn_addr(l2, w, rt, hg);
        const AA aa2 = attn_addr(l2, w + 4, rt, hg);
        unsigned wt = ((qi * 8 + w) * 49 + min(l2, 48)) * 128;
        asm volatile("" : "+v"(wt));
        auto tw = [&]() {
            asm volatile("s_mov_b32 m0, %2\n\ts_nop 0\n\tglobal_load_lds_dword %0, %1" ::"v"(wt), "s"(wo), "i"(Q_DUMMY) : "memory");
            asm volatile("s_mov_b32 m0, %2\n\ts_nop 0\n\tglobal_load_lds_dword %0, %1" ::"v"(wt), "s"(wo + 4 * 49 * 128), "i"(Q_DUMMY) : "memory");
        };
        wait_vm<Q5N>();
        BARL();
        Q6;
        {
            char* tq = smem + W_A + c * 1024 + l2 * 16;
            *reinterpret_cast<u32x4*>(tq) = aa2.a; *reinterpret_cast<u32x2*>(tq + 4096) = aa2.b;
            if (w == 3) {
                tq = smem + Q_TAB + l2 * 16;
                *reinterpret_cast<f32x4*>(tq) = am.c0; *reinterpret_cast<f32x4*>(tq + 1024) = am.c1; *reinterpret_cast<u32x4*>(tq + 2048) = am.m;
            }
        }
        BAR();
        if (w == 3) *reinterpret_cast<u32x2*>(smem + ((l2 & 3) * 7 + 4) * GP + 1152) = u32x2{0u, 0u};
        Q7;
        if (w == 0) hs_rmw(hs, hv2, M_R14);
        TT(3);
        BAR();
        Q8;
    }
    const unsigned m0[2] = {am.m[0], am.m[1]}, m1[2] = {am.m[2], am.m[3]};
    const f32x4 c0 = am.c0, c1 = am.c1;
    const float scale = 0.18898223650461361f, log2e = 1.4426950408889634f;
#define FTA(i)
    FTA(0);
    auto rd64 = [&](unsigned a, int g) { return *reinterpret_cast<const s16x4*>(smem + a + g * GP); };
    bf16x8 qf[4], k0[4], k1[4], v0[4], v1[4];
#pragma unroll
    for (int hh = 0; hh < 4; ++hh) {
        qf[hh] = cat(rd64(aa.a[0] + 64, 7 * hh), rd64(aa.a[1] + 64, 7 * hh + 4));
        k0[hh] = cat(rd64(aa.a[0], 28 + 7 * hh), rd64(aa.a[0], 32 + 7 * hh));
        k1[hh] = cat(rd64(aa.a[2], 28 + 7 * hh), rd64(aa.a[2], 32 + 7 * hh));
        SB();
    }
#pragma unroll
    for (int hh = 0; hh < 4; ++hh) {
        v0[hh] = cat(lds_tr(smem + aa.a[3] + 7 * hh * GP), lds_tr(smem + aa.b[0] + 7 * hh * GP));
        v1[hh] = cat(lds_tr(smem + aa.a[3] + (7 * hh + 4) * GP), lds_tr(smem + aa.b[0] + (7 * hh + 4) * GP));
    }
    SB();
    FTA(1);
#define AB2 1
    if (RS2 && AB2 == 0 && w < 4) { SB(); BARL(); SB(); }
    float l[2][4], mx[2];
#pragma unroll
    for (int hp = 0; hp < 2; ++hp) {
        const f32x4 a0 = MFMA(k0[2 * hp], qf[2 * hp], c0), a1 = MFMA(k1[2 * hp], qf[2 * hp], c1);
        const f32x4 b0 = MFMA(k0[2 * hp + 1], qf[2 * hp + 1], c0), b1 = MFMA(k1[2 * hp + 1], qf[2 * hp + 1], c1);
        float t[4];
#pragma unroll
        for (int i = 0; i < 4; ++i) {
            auto r = __builtin_amdgcn_permlane32_swap(__float_as_uint(vmax(a0[i], a1[i])), __float_as_uint(vmax(b0[i], b1[i])), false, false);
            t[i] = vmax(__uint_as_float(r[0]), __uint_as_float(r[1]));
        }
#pragma unroll
        for (int i = 0; i < 2; ++i) {
            unsigned a = pk2(t[2 * i], t[2 * i + 1]);
            a = pk2(__uint_as_float(a << 16) * scale, __uint_as_float(a & 0xffff0000u) * scale);
            l[hp][2 * i] = __uint_as_float(a << 16);
            l[hp][2 * i + 1] = __uint_as_float(a & 0xffff0000u);
        }
        mx[hp] = vmax(vmax3(l[hp][0], l[hp][1], l[hp][2]), l[hp][3]);
    }
    {
        auto r = __builtin_amdgcn_permlane16_swap(__float_as_uint(mx[0]), __float_as_uint(mx[1]), false, false);
        const float c = vmax(__uint_as_float(r[0]), __uint_as_float(r[1])) * -log2e;
        auto q2 = __builtin_amdgcn_permlane16_swap(__float_as_uint(c), __float_as_uint(c), false, false);
        mx[0] = __uint_as_float(q2[0]); mx[1] = __uint_as_float(q2[1]);
    }
    if (RS2 && AB2 == 1 && w < 4) { SB(); BARL(); SB(); }
    float sm[2];
#pragma unroll
    for (int hp = 0; hp < 2; ++hp) {
#pragma unroll
        for (int i = 0; i < 4; ++i) l[hp][i] = __builtin_amdgcn_exp2f(__builtin_fmaf(l[hp][i], log2e, mx[hp]));
        sm[hp] = (l[hp][0] + l[hp][1]) + (l[hp][2] + l[hp][3]);
    }
    {
        auto r = __builtin_amdgcn_permlane16_swap(__float_as_uint(sm[0]), __float_as_uint(sm[1]), false, false);
        const float c = __builtin_amdgcn_rcpf(__uint_as_float(r[0]) + __uint_as_float(r[1]));
        auto q2 = __builtin_amdgcn_permlane16_swap(__float_as_uint(c), __float_as_uint(c), false, false);
        sm[0] = __uint_as_float(q2[0]); sm[1] = __uint_as_float(q2[1]);
    }
    FTA(2);
    const f32x4 z = {0.f, 0.f, 0.f, 0.f};
    const __amdgpu_buffer_rsrc_t rs_o = __builtin_amdgcn_make_buffer_rsrc(o, 0, 2048 * 448 * 2, 0x00020000);
    const bool top = aa.a[0] != aa.a[1];
#pragma unroll
    for (int hp = 0; hp < 2; ++hp) {
        const unsigned p01 = pk2(l[hp][0] * sm[hp], l[hp][1] * sm[hp]), p23 = pk2(l[hp][2] * sm[hp], l[hp][3] * sm[hp]);
        auto e01 = __builtin_amdgcn_permlane32_swap(p01, p01, false, false);
        auto e23 = __builtin_amdgcn_permlane32_swap(p23, p23, false, false);
        u32x4 ov[2];
#pragma unroll
        for (int e = 0; e < 2; ++e) {
            const int hh = 2 * hp + e;
            u32x4 pw = {e01[e] & m0[0], e23[e] & m0[1], e01[e] & m1[0], e23[e] & m1[1]};
            const bf16x8 pf = __builtin_bit_cast(bf16x8, pw);
            const f32x4 o0 = MFMA(v0[hh], pf, z), o1 = MFMA(v1[hh], pf, z);
            ov[e] = u32x4{pk2(o0[0], o0[1]), pk2(o0[2], o0[3]), pk2(o1[0], o1[1]), pk2(o1[2], o1[3])};
        }
        ov[0][2] = top ? ov[1][0] : ov[0][2]; ov[0][3] = top ? ov[1][1] : ov[0][3];
        __builtin_amdgcn_raw_buffer_store_b128(ov[0], rs_o, aa.b[1], hp * 7 * 2048, 16);
        if (!top) __builtin_amdgcn_raw_buffer_store_b128(ov[1], rs_o, aa.b[1], (hp * 7 + 4) * 2048, 16);
    }
    FTA(4);
    if (w >= 4) {
        int l2 = lane;
        asm volatile("" : "+v"(l2));
        __builtin_assume(l2 >= 0 && l2 < 64);
        wo_init(wq, l2, c);
#pragma unroll 1
        for (int t = 0; t < WE; ++t) wo_req(wq, smem + W_A + t * 4096 + c * 1024);
    } else wait_vm<0>();
    const unsigned hv3 = hs_take<WE>(hv2);
    FTA(5);
    BARL();
    return hv3;
}
__device__ __forceinline__ void oproj(char* smem, u16* __restrict__ o, u16* __restrict__ out, int rt, int rg, int ct, int lane, unsigned ka, unsigned kb) {
    const int fr = lane & 15, fq = lane >> 4;
    const __amdgpu_buffer_rsrc_t rs_o = __builtin_amdgcn_make_buffer_rsrc(o, 0, 2048 * 448 * 2, 0x00020000);
    const unsigned ga = (unsigned)(rt * 56 + fq) * 2048u + (unsigned)(rg * 16 + fr) * 16u;
    bf16x8 fa[14];
#pragma unroll
    for (int ks = 0; ks < 14; ++ks) fa[ks] = __builtin_bit_cast(bf16x8, __builtin_amdgcn_raw_buffer_load_b128(rs_o, ga + ks % 7 * 8192u, ks / 7 * 57344, 16));
    SB();
    const int q = (lane >> 2) & 3, p = lane & 3, s = 2 * (fq & 1) + (q >> 1);
    const char* aw[4];
#pragma unroll
    for (int c = 0; c < 4; ++c) aw[c] = smem + (8 * fq + q) * 128 + ((c ^ s) * 32) + 8 * p;
    f32x4 acc[4];
#pragma unroll
    for (int c = 0; c < 4; ++c) acc[c] = f32x4{0.f, 0.f, 0.f, 0.f};
    constexpr int OA = 9;
    bf16x8 fb[14][4];
#pragma unroll
    for (int ks = 0; ks < 14 + OA; ++ks) {
        if (ks >= OA) {
#pragma unroll
            for (int c = 0; c < 4; ++c) acc[c] = MFMA(fb[ks - OA][c], fa[ks - OA], acc[c]);
        }
        if (ks < 14) {
#pragma unroll
            for (int c = 0; c < 4; ++c) fb[ks][c] = cat(lds_tr(aw[c] + ks * 4096), lds_tr(aw[c] + ks * 4096 + 512));
        }
        SB();
    }
    STAMP(1, 3);
    asm volatile("" ::"v"(ka), "v"(kb), "v"(acc[3]));
    const __amdgpu_buffer_rsrc_t rs_out = __builtin_amdgcn_make_buffer_rsrc(out, 0, 2048 * 896 * 2, 0x00020000);
    const unsigned ob = (unsigned)((rt * 128 + (rg >> 1) * 32 + 2 * (fr & 7) + (rg & 1)) * 896 + ct * 64 + (fr & 8) * 4 + (fq & 1) * 16 + (fq >> 1) * 8) * 2u;
    u32x4 v[2];
#pragma unroll
    for (int pr = 0; pr < 2; ++pr) {
        const f32x4 a = acc[2 * pr], b = acc[2 * pr + 1];
        auto x = __builtin_amdgcn_permlane16_swap(pk2(a[0], a[1]), pk2(b[0], b[1]), false, false);
        auto y = __builtin_amdgcn_permlane16_swap(pk2(a[2], a[3]), pk2(b[2], b[3]), false, false);
        v[pr] = u32x4{x[0], y[0], x[1], y[1]};
    }
    u32x4 p0, p1;
#pragma unroll
    for (int i = 0; i < 4; ++i) {
        p0[i] = __builtin_amdgcn_update_dpp(v[0][i], v[1][i], 0x128, 0xf, 0xc, false);
        p1[i] = __builtin_amdgcn_update_dpp(v[1][i], v[0][i], 0x128, 0xf, 0x3, false);
    }
    st16_out<16>(rs_out, ob, p0);
    st16_out<16>(rs_out, ob + 16 * 1792u, p1);
}

__global__ __launch_bounds__(512, 2) void kf(const u16* xA, const u16* wqkv, const char* xB, const char* Hr, const char* Hi, const char* Cr,
                                          const char* Ci, const char* wo, u16* out, char* ws) {
    __shared__ __attribute__((aligned(16))) char smem[LDS_TOTAL];
    int tid = threadIdx.x, bid = blockIdx.x;
    u64 pc0;
    {
        unsigned part, off;
        asm volatile("s_getpc_b64 %0\n\ts_bfe_u32 %1, %2, 0x30003\n\ts_mulk_i32 %1, %3" : "=&s"(pc0), "=&s"(part), "+s"(bid) : "i"(CODE_PART) : "scc");
        asm volatile("s_mov_b32 m0, %4\n\tv_mad_u32_u24 %0, %1, 12, %2\n\tglobal_load_lds_dword %0, %3" : "=&v"(off), "+v"(tid) : "s"(part), "s"(pc0), "i"(Q_DUMMY) : "memory");
        asm volatile("" : "+s"(bid));
    }
    __builtin_assume(tid >= 0 && tid < 512 && bid >= 0 && bid < 256);
    const int lane = tid & 63, xcd = bid & 7;
    int w = __builtin_amdgcn_readfirstlane(tid >> 6), j = bid >> 3;
    STAMP(0, 0);
    u64 tp0 = __builtin_amdgcn_s_memrealtime(); asm volatile("" : "+s"(tp0));
    const int ks = xcd & 3, rl = j & 3, c6 = j >> 2, r8 = (xcd >> 2) * 4 + rl;
    BL q;
    if (j < 24 && w < 4) {
        unsigned ox = (unsigned)(((ks & 1) * 1024 + r8 * 64) * 896), oh = (unsigned)((c6 < 5 ? c6 * 80 : 384) * 896);
#define KR 1
        q.kc = c6 + KR * rl >= 7 ? c6 + KR * rl - 7 : c6 + KR * rl;
        b_off(q, c6 < 4 ? 79 : 63, lane, w);
        const char *p0 = xB, *p1 = Hr, *p2 = Hi;
        asm volatile("" : "+v"(q.offX), "+v"(q.offH[0]), "+v"(q.offH[1]), "+v"(q.offH[2]));
        asm volatile("" : "+s"(p0), "+s"(p1), "+s"(p2), "+s"(ox), "+s"(oh), "+s"(q.kc));
        q.xg = p0 + ox;
        q.hg = ((ks >> 1) ? p2 : p1) + oh;
        STAMP(0, 7);
        reqH(q, smem, w, 0); reqX(q, smem, w, 0);
        reqH(q, smem, w, 1); reqX(q, smem, w, 1);
        asm volatile("" ::"s"(xA), "s"(wqkv), "s"(Cr), "s"(Ci), "s"(wo), "s"(out), "s"(ws));
    }
    asm volatile("" : "+s"(w), "+s"(j));
    __builtin_assume(w >= 0 && w < 8 && j >= 0 && j < 32);
    if (w == 7) *reinterpret_cast<u32x2*>(smem + L_GO) = u32x2{0u, 0u};
    if (w == 6 && lane == 0) __hip_atomic_store((unsigned*)(ws + (24 << 20)) + bid * 32, (unsigned)tp0, __ATOMIC_RELAXED, __HIP_MEMORY_SCOPE_AGENT);
    Hs hs;
    hs.base = (const unsigned*)(ws + WS_HS); hs.off = 0; hs.snap = 0;
    int ro = 0, rg = 0, ct = 0;
    bool duty = true;
    u64 again = 0, mr = M_R14;
    unsigned ka = 0, kb = 0;
    int wb = W_0;
    if (j < 24) {
        u16* ab = (u16*)ws;
        u16* px = (u16*)(ws + WS_PX);
        const int rbl = j & 7, cg = j >> 3, rb = (xcd >> 2) * 8 + rbl;
#define DU 1
        duty = DU ? (j & 19) != 3 : j >= 4;
        const int un = DU ? j - min(c6, 4) : j - 4, uc = un >= 14 ? un - 14 : un;
        ro = xcd * 2 + (un >= 14); ct = (uc >> 1) & 7; rg = (uc & 1) * 4 + (w & 3);
        const int g1 = ks * 3 + cg;
        if (w < 4) {
            const char* cgp = ((ks >> 1) ? Ci : Cr) + (long)rb * (128 * 4096) + (ks & 1) * 2048;
            b_load(smem, q, cgp, (const char*)ab + g1 * (16 * B_SL), (u64)(((cg == 0 ? TD0 : cg == 1 ? TD1 : TD2) + rbl * 0x11111111u) & 0x77777777u) << 32 |
                   (((cg == 0 ? TC0 : cg == 1 ? TC1 : TC2) + rbl * 0x11111111u) & 0x77777777u), ks, cg == 2,
                   wo + (duty ? ct : 0) * 128, duty, hs, lane, w, r8 * 2 + (c6 & 1));
        } else {
            const int cw = w - 4;
            asm volatile("" ::"s"(xB), "s"(Hr), "s"(Hi));
            hs.off = hs_off(lane, g1 * 256, HS_X + ((rb * 3 + cg) * 4 + cw) * 256, ro, uc & 15);
            unsigned v1 = 0;
            hs_rmw(hs, v1, w == 5 ? M_LO | M_HI | M_XW | M_XR | M_OW : M_XW | M_XR);
            STAMP(0, 7);
            auto post = [&]() {
                if (w == 5) *reinterpret_cast<u32x2*>(smem + L_KEY + lane * 8) = u32x2{hs.off, hs_take<0>(v1)};
            };
            const unsigned dbase = (unsigned)((g1 * 16 + r8 * 2) * B_SL + (c6 & 1) * (c6 < 4 ? 80 : 64) * 64);
            const unsigned sg = (ks >> 1) ? 0x80008000u : 0u;
            if (c6 < 4 && !(cw & 1)) ab_mma<3>(smem, dbase, sg, lane, cw >> 1, 0, ab, post);
            else ab_mma<2>(smem, dbase, sg, lane, cw >> 1, (cw & 1) * (c6 < 4 ? 3 : 2), ab, post);
            hs.snap = hs_take<63>(v1);
            hs_add(hs, 1ull << (36 + ks));
            const unsigned pxb = (unsigned)(rb * 3 + cg) * 131072u, ob0 = (unsigned)((rb * 128 + ks * 16) * 896 + 448 + cg * 160) * 2u;
            if (cg == 2) y_mma<4>(smem, lane, cw, px, pxb, ks, out, ob0, hs);
            else y_mma<5>(smem, lane, cw, px, pxb, ks, out, ob0, hs);
            duty = false;
        }
    } else {
        const int qi = j - 24;
#define HGM 0x3102
        const int hg = HGM >> (4 * ks) & 3, rt = (xcd >> 2) * 8 + qi;
#define XT4 0x5401
        int kst = qi + (XT4 >> (4 * hg) & 15); kst = kst >= 7 ? kst - 7 : kst;
        ro = xcd * 2 + 1; ct = 3 + (qi >> 1); rg = (qi & 1) * 4 + (w & 3); duty = w < 4;
        const int co = 6 + qi;
        WO wq;
        wq.g = wo + ct * 128;
        const unsigned hv = qkv_attn(smem, xA, wqkv, wo, ws, rt, hg, tid, w, hs, kst, ro, co,
                                     (u64)(((ks >> 1) ? Ci : Cr) + (long)((xcd >> 2) * 8 + qi) * (128 * 4096) + (ks & 1) * 2048), qi, wq);
        STAMP(0, 21);
        if (w == 0) {
            hs_rmw(hs, ka, 1ull << hg | M_OW, lane == hg);
            if (!hs_new(hs, hv, mr)) again = 1ull << hg;
        }
        if (w >= 4) {
#pragma unroll 1
            for (int t = WE; t < 14; ++t) wo_req(wq, smem + W_A + t * 4096 + (w & 3) * 1024);
            wait_vm<0>();
        }
        BARL();
        wb = W_A;
    }
    if (duty) {
        volatile LDS_AS unsigned* go = (volatile LDS_AS unsigned*)(smem + L_GO);
        if (w == 0) { if (j < 24) { hs_rmw(hs, ka, M_OW); __builtin_amdgcn_s_sleep(PD2); } hs_wait2(hs, M_OW, M_OW, 0u, ka, kb); *go = 1u; }
        else lds_wait<SL2>(L_GO);
        asm volatile("" ::: "memory");
        STAMP(1, 0);
        if (w == 0) hs_add(hs, M_OR);
        oproj(smem + wb, (u16*)(ws + WS_O), out, ro, rg, ct, lane, ka, kb);
        if (again) { hs_wait(hs, mr, mr, 0u); hs_add(hs, again); }
    }
    {
        int best = 0;
#pragma unroll 1
        for (int i = 0; i < 4; ++i) {
            const unsigned v = __hip_atomic_load((unsigned*)(ws + (24 << 20)) + (i * 64 + lane) * 32, __ATOMIC_RELAXED, __HIP_MEMORY_SCOPE_AGENT);
            const int d = (int)(v - (unsigned)tp0);
            if (d > -5000 && d < 5000) best = MODE ? max(best, d) : min(best, d);
        }
#pragma unroll
        for (int o = 32; o > 0; o >>= 1) { const int x = __shfl_xor(best, o); best = MODE ? max(best, x) : min(best, x); }
        best = __builtin_amdgcn_readfirstlane(best);
#pragma unroll 1
        while ((int)((unsigned)__builtin_amdgcn_s_memrealtime() - (unsigned)tp0) - best < TW) __builtin_amdgcn_s_sleep(2);
    }
    STAMP(1, 24);
}

extern "C" void kernel_launch(void* const* d_in, const int* in_sizes, int n_in, void* d_out, int out_size,
                              void* d_ws, size_t ws_size, hipStream_t stream) {
    kf<<<256, 512, 0, stream>>>((const u16*)d_in[0], (const u16*)d_in[1], (const char*)d_in[3], (const char*)d_in[6], (const char*)d_in[7],
                                (const char*)d_in[4], (const char*)d_in[5], (const char*)d_in[2], (u16*)d_out, (char*)d_ws);
}
```

```cpp
#include <hip/hip_runtime.h>
#include <hip/hip_bf16.h>

typedef unsigned short u16;
typedef __attribute__((ext_vector_type(8))) short bf16x8;
typedef __attribute__((ext_vector_type(4))) short s16x4;
typedef __attribute__((ext_vector_type(4))) float f32x4;
typedef __attribute__((ext_vector_type(2))) __bf16 bf2;
typedef __attribute__((ext_vector_type(2))) unsigned u32x2;
typedef __attribute__((ext_vector_type(4))) unsigned u32x4;

#define LDS_AS __attribute__((address_space(3)))
#define GLB_AS __attribute__((address_space(1)))

constexpr int D = 448;

#define STAMP(kern, idx)
#define STAMPC(kern, idx)

typedef __attribute__((ext_vector_type(2))) float f32x2;
__device__ __forceinline__ unsigned pk2(float a, float b) {
    f32x2 v = {a, b};
    return __builtin_bit_cast(unsigned, __builtin_convertvector(v, bf2));
}
__device__ __forceinline__ void glds16a(const void* sbase, unsigned voff, const char* l) {
    const unsigned la = (unsigned)(size_t)(LDS_AS const char*)l;
    asm volatile("s_mov_b32 m0, %2\n\ts_nop 0\n\tglobal_load_lds_dwordx4 %0, %1" ::"v"(voff), "s"(sbase), "s"(la) : "memory");
}
__device__ __forceinline__ float vmax(float a, float b) { return __builtin_elementwise_maximum(a, b); }
__device__ __forceinline__ float vmax3(float a, float b, float c) { return vmax(vmax(a, b), c); }
__device__ __forceinline__ bf16x8 lds_rd128(const char* p) { return *reinterpret_cast<const bf16x8*>(p); }
__device__ __forceinline__ s16x4 lds_tr(const char* p) {
    return __builtin_amdgcn_ds_read_tr16_b64_v4i16((LDS_AS s16x4*)p);
}
__device__ __forceinline__ bf16x8 cat(s16x4 a, s16x4 b) {
    bf16x8 r;
    r[0] = a[0]; r[1] = a[1]; r[2] = a[2]; r[3] = a[3];
    r[4] = b[0]; r[5] = b[1]; r[6] = b[2]; r[7] = b[3];
    return r;
}
#define MFMA(a, b, c) __builtin_amdgcn_mfma_f32_16x16x32_bf16((a), (b), (c), 0, 0, 0)
template <int N> __device__ __forceinline__ void wait_vm() { asm volatile("s_waitcnt vmcnt(%0)" ::"i"(N) : "memory"); }
#define SB() __builtin_amdgcn_sched_barrier(0)
template <int NP, int NM> __device__ __forceinline__ void ilv() {
#pragma unroll
    for (int i = 0; i < NP; ++i) {
        __builtin_amdgcn_sched_group_barrier(0x8, 1, 0);
        __builtin_amdgcn_sched_group_barrier(0x100, 1, 0);
    }
    if constexpr (NM > 0) __builtin_amdgcn_sched_group_barrier(0x8, NM, 0);
}
#define BAR()                                               \
    do {                                                    \
        asm volatile("s_waitcnt lgkmcnt(0)" ::: "memory");  \
        __builtin_amdgcn_s_barrier();                       \
        asm volatile("" ::: "memory");                      \
    } while (0)
#define BARL()                                              \
    do {                                                    \
        asm volatile("" ::: "memory");                      \
        __builtin_amdgcn_s_barrier();                       \
        asm volatile("" ::: "memory");                      \
    } while (0)

#define BARK() do { __builtin_amdgcn_s_waitcnt(0xc07f); asm volatile("" ::: "memory"); __builtin_amdgcn_s_barrier(); asm volatile("" ::: "memory"); SB(); } while (0)
__device__ __forceinline__ int kperm(int r) { return (r & ~12) | ((r & 4) << 1) | ((r & 8) >> 1); }

constexpr int LDS_TOTAL = 163840;
constexpr int WS_O = 4 << 20, WS_PX = 8 << 20, WS_HS = 16 << 20;
#define CODEMAX 41984u
constexpr unsigned CODE_MAX = CODEMAX;

typedef unsigned long long u64;
struct Hs {
    const unsigned* base;
    unsigned off, snap;
};
constexpr int HS_O = 16 * 256, HS_X = 32 * 256;
constexpr u64 M_LO = 0xffffull, M_HI = 0xffff0000ull, M_OW = 0xfull << 48, M_OR = 1ull << 52, M_R14 = 0x3fffull << 16;
constexpr u64 M_XW = 0xfull << 32, M_XR = 0xf0ull << 32;
__device__ __forceinline__ unsigned hs_off(int lane, int b, int bx, int ro, int co) {
    const int bo = HS_O + ro * 256 - 48 * 4;
    return (unsigned)(lane * 4 + (lane < 32 ? b : lane < 48 ? bx - 32 * 4 : lane == 52 ? bo + 48 + co * 4 : bo));
}
template <int N> __device__ __forceinline__ unsigned hs_take(unsigned v) {
    unsigned r;
    asm volatile("s_waitcnt vmcnt(%2)\n\tv_mov_b32 %0, %1" : "=v"(r) : "v"(v), "i"(N) : "memory");
    return r;
}
__device__ __forceinline__ void hs_rmw(const Hs& h, unsigned& v, u64 m, unsigned d = 0u) {
    asm volatile("s_mov_b64 exec, %4\n\tglobal_atomic_add %0, %1, %2, %3 sc0 sc1\n\ts_mov_b64 exec, -1" : "+v"(v) : "v"(h.off), "v"(d), "s"(h.base), "s"(m) : "memory");
}
__device__ __forceinline__ void hs_add(const Hs& h, u64 m) {
    const unsigned one = 1u;
    asm volatile("s_mov_b64 exec, %3\n\tglobal_atomic_add %0, %1, %2 sc1\n\ts_mov_b64 exec, -1" ::"v"(h.off), "v"(one), "s"(h.base), "s"(m) : "memory");
}
__device__ __forceinline__ bool hs_new(const Hs& h, unsigned v, u64 m) { return (__ballot(v == h.snap) & m) == 0; }
#define PSL 2
__device__ __forceinline__ void hs_wait(const Hs& h, u64 m, u64 e, unsigned d) {
#pragma unroll 1
    for (unsigned spin = 0; spin < 40000u; ++spin) {
        unsigned v = 0;
        hs_rmw(h, v, e, d);
        if (hs_new(h, hs_take<0>(v), m)) return;
        __builtin_amdgcn_s_sleep(PSL);
    }
}
#define PD2 5
__device__ __forceinline__ void hs_wait2(const Hs& h, u64 m, u64 e, unsigned d, unsigned& a, unsigned& b) {
    unsigned n = 40000u;
    asm volatile("s_mov_b64 exec, %8\n"
                 "1:\n\tglobal_atomic_add %1, %3, %4, %5 sc0 sc1\n\ts_waitcnt vmcnt(1)\n\tv_cmp_eq_u32 vcc, %0, %6\n\ts_and_b64 vcc, vcc, %7\n\ts_cbranch_scc0 2f\n\t"
                 "global_atomic_add %0, %3, %4, %5 sc0 sc1\n\ts_waitcnt vmcnt(1)\n\tv_cmp_eq_u32 vcc, %1, %6\n\ts_and_b64 vcc, vcc, %7\n\ts_cbranch_scc0 2f\n\t"
                 "s_sub_u32 %2, %2, 1\n\ts_cbranch_scc0 1b\n"
                 "2:\n\ts_mov_b64 exec, -1"
                 : "+&v"(a), "+&v"(b), "+&s"(n) : "v"(h.off), "v"(d), "s"(h.base), "v"(h.snap), "s"(m), "s"(e) : "memory", "vcc", "scc");
}
template <int POL> __device__ __forceinline__ void st16_out(__amdgpu_buffer_rsrc_t r, unsigned off, u32x4 v) {
    __builtin_amdgcn_raw_buffer_store_b128(v, r, off, 0, POL);
}
__device__ __forceinline__ void st16_sc1(__amdgpu_buffer_rsrc_t r, unsigned off, u32x4 v) {
    __builtin_amdgcn_raw_buffer_store_b128(v, r, off, 0, 16);
}
__device__ __forceinline__ void glds16m(const void* sbase, unsigned voff, const char* l, u64 mask) {
    const unsigned la = (unsigned)(size_t)(LDS_AS const char*)l;
    asm volatile("s_mov_b32 m0, %2\n\ts_mov_b64 exec, %3\n\tglobal_load_lds_dwordx4 %0, %1\n\ts_mov_b64 exec, -1" ::"v"(voff), "s"(sbase), "s"(la), "s"(mask) : "memory");
}
__device__ __forceinline__ void glds16c(const void* sbase, unsigned voff, const char* l) {
    const unsigned la = (unsigned)(size_t)(LDS_AS const char*)l;
    asm volatile("s_mov_b32 m0, %2\n\ts_nop 0\n\tglobal_load_lds_dwordx4 %0, %1 sc1" ::"v"(voff), "s"(sbase), "s"(la) : "memory");
}
__device__ __forceinline__ void glds16mc(const void* sbase, unsigned voff, const char* l, u64 mask) {
    const unsigned la = (unsigned)(size_t)(LDS_AS const char*)l;
    asm volatile("s_mov_b32 m0, %2\n\ts_mov_b64 exec, %3\n\tglobal_load_lds_dwordx4 %0, %1 sc1\n\ts_mov_b64 exec, -1" ::"v"(voff), "s"(sbase), "s"(la), "s"(mask) : "memory");
}
template <int SL> __device__ __forceinline__ void lds_wait(unsigned a) {
    unsigned n = 1000000u, v;
    asm volatile("1:\n\ts_sleep %3\n\tds_read_b32 %0, %2\n\ts_sub_u32 %1, %1, 1\n\ts_cbranch_scc1 2f\n\ts_waitcnt lgkmcnt(0)\n\tv_cmp_eq_u32 vcc, 0, %0\n\ts_cbranch_vccnz 1b\n"
                 "2:\n\ts_waitcnt lgkmcnt(0)" : "=&v"(v), "+s"(n) : "v"(a), "i"(SL) : "memory", "vcc", "scc");
}

constexpr int A_SL = 16384, B_R0 = 6 * A_SL, B_SL = 20480, X_SL = 8192, H_SL = 10240, H_0 = 11 * X_SL, L_KEY = H_0 + 7 * H_SL, L_GO = 163328, W_0 = A_SL;
constexpr int CODE_PART = (CODE_MAX - 6144) / 7 / 128 * 128;
#define TT(i)
struct WO { const char* g; u64 pat; unsigned off, hi; };
__device__ __forceinline__ void wo_init(WO& q, int lane, int w) {
    q.g += w * (8 * 896);
    q.pat = 0ull;
#pragma unroll
    for (int i = 0; i < 21; ++i) q.pat |= (u64)(i % 7 == 6 ? 7 : (i % 7 + 1) / 2) << (3 * i);
    q.pat >>= 6 * w;
    q.hi = (unsigned)(lane >> 5) * (3 * 896);
    q.off = (unsigned)(((lane >> 5) * 16 + ((lane >> 3) & 3)) * 896 + ((lane & 7) ^ ((w & 1) * 4 + ((lane >> 4) & 1) * 2)) * 16);
    asm volatile("" : "+v"(q.off), "+v"(q.hi));
}
__device__ __forceinline__ void wo_req(WO& q, const char* l) {
    glds16a(q.g - ((unsigned)q.pat & 3) * (4 * 896), q.off + ((unsigned)q.pat & 4) * q.hi, l);
    q.g += 32 * 896; q.pat >>= 3;
}
#define SL1 1
#define SL2 1
#define N4 3
#define N5 4
#define N6 5
#define KA 3
#define V1 1
#define PDG 1
#define DEF 2
#define TC0 0x75643210u
#define TC1 0x67540132u
#define TD0 0x76543210u
#define TD1 TD0
#define TD2 TD0
#define TQ 0x76543210u
#define TC2 0x54763201u
#define LP 2
#define WP1 3
#define WP2 10
#define WE 2
struct BL {
    const char *xg, *hg;
    unsigned offX, offH[3];
    int kc;
};
__device__ __forceinline__ void reqX(BL& q, char* smem, int w, int sx) {
    const char* gx = q.xg + q.kc * 128;
    const char* la = smem + sx * X_SL + w * 2048;
    glds16a(gx, q.offX, la);
    glds16a(gx + 8 * 896, q.offX, la + 1024);
    q.kc = q.kc == 6 ? 0 : q.kc + 1;
}
__device__ __forceinline__ void reqH(BL& q, char* smem, int w, int k) {
    const char* gh = q.hg + q.kc * 128;
    const char* lh = smem + H_0 + k * H_SL + w * 2560;
    glds16a(gh, q.offH[0], lh);
    glds16a(gh, q.offH[1], lh + 1024);
    glds16m(gh, q.offH[2], lh + 2048, 0xffffffffull);
}
__device__ __forceinline__ void b_off(BL& q, int hn, int lane, int w) {
    const int r8 = lane >> 3, c = (lane & 7) ^ r8;
    q.offX = (unsigned)((w * 16 + r8) * 896 + c * 16);
#pragma unroll
    for (int i = 0; i < 3; ++i) q.offH[i] = (unsigned)(min(w * 20 + i * 8 + r8, hn) * 896 + (c ^ ((w & 1) * 4)) * 16);
}
__device__ __forceinline__ void b_load(char* smem, BL& q, const char* cg, const char* abg, u64 tb, int ks,
                                       bool narrow, const char* wog, bool duty, Hs& hs, int lane, int w, int pm) {
    const int r8 = lane >> 3, c = (lane & 7) ^ r8;
    unsigned offA[4];
#pragma unroll
    for (int i = 0; i < 4; ++i)
        offA[i] = (unsigned)(((w >> 1) * 64 + (((w & 1) * 2 + (i >> 1) + ks) & 3) * 16 + (i & 1) * 8 + r8) * 4096 + c * 16);
    const int rb0 = w * (narrow ? 32 : 40);
    unsigned offB = (unsigned)((c >> 2) * 10240 + (rb0 + r8) * 64 + (c & 3) * 16);
    u64 mB = narrow ? 1ull : ~0ull;
    int sa = 3;
    u64 tc = tb | 0x8888888800000000ull;
    const unsigned t3 = (unsigned)tb;
    auto reqC = [&]() {
        const char* g = cg + ((unsigned)tc & 15) * 128;
        const char* la = smem + sa * A_SL + w * 4096;
#pragma unroll
        for (int i = 0; i < 4; ++i) glds16a(g, offA[i], la + i * 1024);
        sa = sa == 5 ? 0 : sa + 1; tc >>= 4;
    };
    int sb = 0, lb0 = B_R0 + rb0 * 128;
    tb = tb << 1 | 0x1111111100000000ull;
    auto reqB = [&]() {
        const char* g = abg + ((unsigned)tb & 15) * B_SL;
        const char* lb = smem + lb0 + sb * B_SL;
#pragma unroll
        for (int i = 0; i < 4; ++i) glds16c(g + i * 512, offB, lb + i * 1024);
        glds16mc(g + 2048, offB, lb + 4096, mB);
        sb = sb == 2 ? 0 : sb + 1; tb >>= 4;
    };
    auto step = [&]() { reqB(); reqC(); wait_vm<13>(); BARL(); };
    u64 tt[11] = {0, 0, 0, 0, 0, 0, 0, 0, 0, 0, 0};
#pragma unroll 1
    for (int k = 2; k < 7; ++k) {
        reqH(q, smem, w, k); reqX(q, smem, w, k == 6 ? 10 : k);
        wait_vm<10>();
        BARL();
    }
    q.xg += 512 * 896;
    auto reqX2 = [&](int a, int b) {
#pragma unroll
        for (int i = a; i < b; ++i) reqX(q, smem, w, i == 6 ? 10 : i);
    };
    reqX2(0, N4);
    wait_vm<5 + 2 * N4>();
    BARL();
    reqX2(N4, N5);
    wait_vm<2 * N5>();
    BARL();
    TT(0);
    reqX2(N5, N6);
    unsigned own = (lane & 15) == pm, hv = 0, hw = 0;
    u64 m3 = 3u << (t3 << 1 & 14) | 3u << (t3 >> 3 & 14) | 3u << (t3 >> 7 & 14);
    asm volatile("" : "+v"(offA[0]), "+v"(offA[1]), "+v"(offA[2]), "+v"(offA[3]), "+v"(offB), "+v"(own));
    asm volatile("" : "+s"(mB), "+s"(m3), "+s"(abg), "+s"(cg), "+s"(lb0), "+s"(tb), "+s"(tc));
    if (w == 0) {
        const u32x2 t = *reinterpret_cast<const u32x2*>(smem + L_KEY + lane * 8);
        hs.off = t[0]; hs.snap = t[1];
    }
    wait_vm<2 * (N6 - KA)>();
    BARL();
    TT(1);
    reqX2(N6, 7);
    reqC();
    if (V1) reqC();
    wait_vm<4 + 4 * V1>();
    BARL();
    TT(2);
    if (w == 0) hs_add(hs, 1ull << pm);
    if (!V1) reqC();
#define PDA 5
    if (PDA && w == 0) { __builtin_amdgcn_s_sleep(PDA); hs_rmw(hs, hv, M_LO); }
    BARL();
    TT(3);
    if (w == 0) {
        if (!PDA) hs_rmw(hs, hv, M_LO);
        __builtin_amdgcn_s_sleep(PDG);
        hs_wait2(hs, m3, M_LO, own, hv, hw);
        *(volatile LDS_AS unsigned*)(smem + L_GO + 4) = 1u;
    } else lds_wait<SL1>(L_GO + 4);
    TT(4);
    reqB(); reqB();
    unsigned h2 = 0;
    if (w == 0) hs_rmw(hs, h2, M_LO);
    reqC();
    wait_vm<9>();
    asm volatile("" ::"v"(hv), "v"(hw));
    BARL();
    reqB(); reqC();
    {
        const unsigned v = hs_take<13>(h2);
        if (w == 0) { if (!hs_new(hs, v, M_LO)) hs_wait(hs, M_LO, M_LO, own); }
    }
    BARL();
    if (w == 0) hs_add(hs, 0x10001ull << pm | (duty ? M_OR : 0ull));
#pragma unroll 1
    for (int i = 1; i < 4; ++i) step();
    if (w == 0) hs_rmw(hs, h2, M_LO | M_HI, own);
    step();
    if (w == 0) { if (!hs_new(hs, hs_take<9>(h2), M_HI)) hs_wait(hs, M_HI, M_HI, 0u); }
#pragma unroll 1
    for (int i = 5; i < 13; ++i) step();
    WO wq;
    wq.g = wog;
    wo_init(wq, lane, w);
    reqB();
    if (duty) {
#pragma unroll
        for (int n = 0; n < 14; ++n) {
            if (n == WP1) { wait_vm<9 + WP1>(); BARL(); }
            if (n == WP2) { wait_vm<WP2>(); BARL(); }
            wo_req(wq, smem + W_0 + n * 4096 + w * 1024);
        }
    } else {
        wait_vm<9>(); BARL();
        wait_vm<0>(); BARL();
    }
    wait_vm<0>();
    BARL();
}
template <int NE, class F>
__device__ __forceinline__ void ab_mma(char* smem, unsigned dbase, unsigned sg, int lane, int sh, int ef, u16* ab, F post) {
    const int fr = lane & 15, fq = lane >> 4;
    const int z0 = fr * 128 + ((fq ^ (fr & 7)) * 16), z1 = z0 ^ 64;
    unsigned a0 = sh * 4096 + z0, a1 = sh * 4096 + z1, b0 = H_0 + ef * 2048 + z0, b1 = H_0 + ef * 2048 + z1;
    unsigned doff = dbase + (unsigned)(sh * 10240 + (ef * 16 + fr) * 64 + ((fq & 1) * 2 + (fq >> 1)) * 16);
    asm volatile("" : "+v"(a0), "+v"(a1), "+v"(b0), "+v"(b1), "+v"(doff));
    const char *sa0 = smem + a0, *sa1 = smem + a1, *sb0 = smem + b0, *sb1 = smem + b1;
    const __amdgpu_buffer_rsrc_t rs = __builtin_amdgcn_make_buffer_rsrc(ab, 0, 192 * B_SL, 0x00020000);
    f32x4 acc[NE][2];
    bf16x8 xf[2][2][2], hf[2][2][NE];
    auto rd = [&](int k, int i) {
        const int h = i >= 2 + NE, n = i - h * (2 + NE);
        if (n < 2) xf[k & 1][h][n] = lds_rd128((h ? sa1 : sa0) + (k == 6 ? 10 : k) * X_SL + n * 2048);
        else hf[k & 1][h][n - 2] = lds_rd128((h ? sb1 : sb0) + k * H_SL + (n - 2) * 2048);
    };
#pragma unroll 1
    for (int p = 0; p < 2; ++p) {
#pragma unroll
        for (int a = 0; a < NE; ++a) {
            acc[a][0] = f32x4{0.f, 0.f, 0.f, 0.f}; acc[a][1] = f32x4{0.f, 0.f, 0.f, 0.f};
            asm volatile("" : "+v"(acc[a][0]), "+v"(acc[a][1]));
        }
#pragma unroll
        for (int k = 0; k < 8; ++k) {
            if (k == 0) BARL();
            else if (k == KA) { if (p) wait_vm<0>(); BARL(); }
            else if (k == 7) { if (p) BAR(); }
            else if (!p) { if (k == 6) { post(); BAR(); } else BARL(); }
            if (k == 0 && p == 0) STAMP(0, 1);
            SB();
#pragma unroll
            for (int i = 0; i < 4 * NE; ++i) {
                const int h = i >= 2 * NE, n = i - h * 2 * NE;
                if (k == 7) acc[n >> 1][n & 1] = MFMA(xf[0][h][n & 1], hf[0][h][n >> 1], acc[n >> 1][n & 1]);
                else if (k) asm volatile("v_mfma_f32_16x16x32_bf16 %0, %1, %2, %0" : "+v"(acc[n >> 1][n & 1]) : "v"(xf[~k & 1][h][n & 1]), "v"(hf[~k & 1][h][n >> 1]));
                SB();
                if (k < 7 && i < 4 + 2 * NE) rd(k, i);
                SB();
            }
        }
#pragma unroll
        for (int mi = 0; mi < NE; ++mi) {
            const f32x4 a = acc[mi][0], b = acc[mi][1];
            auto x = __builtin_amdgcn_permlane16_swap(pk2(a[0], a[1]) ^ sg, pk2(b[0], b[1]) ^ sg, false, false);
            auto y = __builtin_amdgcn_permlane16_swap(pk2(a[2], a[3]) ^ sg, pk2(b[2], b[3]) ^ sg, false, false);
            st16_sc1(rs, doff + mi * 1024u, u32x4{x[0], y[0], x[1], y[1]});
        }
        SB();
        doff += B_SL;
    }
}
template <int NF>
__device__ __forceinline__ void y_mma(char* smem, int lane, int cw, u16* px, unsigned pxb, int ks, u16* out, unsigned ob0, Hs& hs) {
    const int fr = lane & 15, fq = lane >> 4;
    const int mh = cw >> 1, nh = cw & 1;
    f32x4 acc[5][4];
#pragma unroll
    for (int a = 0; a < NF; ++a)
#pragma unroll
        for (int b = 0; b < 4; ++b) { acc[a][b] = f32x4{0.f, 0.f, 0.f, 0.f}; asm volatile("" : "+v"(acc[a][b])); }
    const int z0 = fr * 128 + ((fq ^ (fr & 7)) * 16), z1 = z0 ^ 64;
    unsigned a0 = 3 * A_SL + mh * 8192 + z0, a1 = 3 * A_SL + mh * 8192 + z1, b0 = B_R0 + nh * (NF * 2048) + z0, b1 = B_R0 + nh * (NF * 2048) + z1;
    unsigned pl = pxb + (unsigned)(cw * 8192 + lane * 16), ph = pl + (unsigned)(6144 - 512 - lane * 8);
    if (NF == 5) asm volatile("" : "+v"(ph));
    unsigned ob = ob0 + (unsigned)((mh * 64 + fr) * 896 + nh * (NF * 16) + (fq & 1) * 16 + (fq >> 1) * 8) * 2u;
    const unsigned one = fq == (fr >> 2) ? 0x3f80u << ((fr & 1) * 16) : 0u, d0 = (fr & 2) ? 0u : one, d1 = (fr & 2) ? one : 0u;
    u32x4 s0 = {d0, d1, 0u, 0u}, s1 = {0u, 0u, d0, d1};
    unsigned own = lane == 32 + ks;
    asm volatile("" : "+v"(a0), "+v"(a1), "+v"(b0), "+v"(b1), "+v"(pl), "+v"(ob), "+v"(s0), "+v"(s1), "+v"(own));
    const char *sa0 = smem + a0, *sa1 = smem + a1, *sb0 = smem + b0, *sb1 = smem + b1;
    const __amdgpu_buffer_rsrc_t rs = __builtin_amdgcn_make_buffer_rsrc(px, 0, 48 * 4 * 32768, 0x00020000);
    const __amdgpu_buffer_rsrc_t rs_out = __builtin_amdgcn_make_buffer_rsrc(out, 0, 2048 * 896 * 2, 0x00020000);
    bf16x8 cf[2][4], xf[2][NF];
    auto rdx = [&](int mi, int k, int st) { xf[k][mi] = lds_rd128((k ? sb1 : sb0) + st * B_SL + mi * 2048); };
    auto rdc = [&](int l, int k, int st) { cf[k][l] = lds_rd128((k ? sa1 : sa0) + st * A_SL + l * 2048); };
    auto mf = [&](int mi, int l, int k) {
        asm volatile("v_mfma_f32_16x16x32_bf16 %0, %1, %2, %0" : "+v"(acc[mi][l]) : "v"(cf[k][l]), "v"(xf[k][mi]));
        SB();
    };
    int da = -3 * A_SL;
    auto head = [&](int st, int lo) {
        int n = 0;
        __builtin_amdgcn_s_waitcnt(0xc47f);
        SB();
#pragma unroll
        for (int i = 4 * lo; i < 16; ++i) {
            mf(((i >> 1) ^ (i >> 2)) & 1, i >> 2, (i ^ (i >> 1)) & 1);
            if (n < 2 * NF - 4) rdx(2 + (n >> 1), n & 1, st);
            if (st == 2 && n == 8) sa0 += da;
            if (st == 2 && n == 10) { sa1 += da; da = -da; }
            ++n;
            SB();
        }
        if (NF == 5) {
#pragma unroll
            for (int i = 0; i < 8 - 2 * lo; ++i) mf(2, 3 - (i >> 1), (i ^ (i >> 1)) & 1);
        }
    };
    auto rest = [&](int st, int lo) {
#pragma unroll
        for (int i = 4 * lo; i < 16; ++i) {
            mf(NF - 2 + (((i >> 1) ^ (i >> 2)) & 1), i >> 2, (i ^ (i >> 1)) & 1);
            if ((i & 3) >= 2) rdc(i >> 2, ~i & 1, st);
            else if (i - 4 * lo < 8) rdx((i >> 2) - lo, i & 1, st);
            else if (lo) rdc(0, i & 1, st);
            SB();
        }
    };
    STAMP(0, 4);
    BARL();
    STAMP(0, 5); STAMPC(0, 3);
#pragma unroll
    for (int i = 0; i < 16; ++i) {
        if ((i & 3) >= 2) rdc(i >> 2, i & 1, 0);
        else if (i < 8) rdx(i >> 2, i & 1, 0);
        SB();
    }
#pragma unroll 1
    for (int t = 0; t < 4; ++t) {
        head(0, 0);
        wait_vm<0>();
        BARK();
        rest(1, 0); head(1, 0);
        BARK();
        rest(2, 0); head(2, 0);
        BARK();
        rest(0, 0);
    }
    bf16x8 kf[4];
    unsigned ka = 0, kb = 0;
    head(0, 0); BARK();
    rest(1, 0); head(1, 1); BARK();
    kf[0] = cf[0][0]; kf[1] = cf[1][0];
    rest(2, 1); head(2, 1); BARK();
    hs_rmw(hs, kb, M_XR);
    kf[2] = cf[0][0]; kf[3] = cf[1][0];
    rest(0, 1);
    STAMP(0, 18); STAMPC(0, 4);
    const int pw = ks * 32768;
    auto put = [&](int p, int h) {
        const int sl = 2 * (p - 1) + h;
        const f32x4 a = acc[h < 2 ? 2 * h : NF - 1][p], b = acc[h < 2 ? 2 * h + 1 : 0][p];
        if (h < 2) __builtin_amdgcn_raw_buffer_store_b128(u32x4{pk2(a[0], a[1]), pk2(a[2], a[3]), pk2(b[0], b[1]), pk2(b[2], b[3])}, rs, pl + (sl & 3) * 1024u, pw + (sl >> 2 << 12), 16);
        else __builtin_amdgcn_raw_buffer_store_b64(u32x2{pk2(a[0], a[1]), pk2(a[2], a[3])}, rs, ph + p * 512u, pw, 16);
    };
    constexpr int NG = 2 * NF - 4, NL = 12 + 3 * NG;
#pragma unroll
    for (int i = 0; i < NL + 2 * NF; ++i) {
        const int j = i - 12, l = i < 12 ? 1 + (i >> 2) : i < NL ? 1 + j / NG : 0, mi = i < 12 ? (i >> 1) & 1 : i < NL ? 2 + j % NG / 2 : (i - NL) >> 1;
        acc[mi][l] = MFMA(cf[i & 1][l], xf[i & 1][mi], acc[mi][l]);
        SB();
        if (i < NG) rdx(2 + (i >> 1), i & 1, 0);
        const int e = i - 2;
        if (e == 4 || e == 8 || e == 12) put(e >> 2, 0);
        if (e > 12 && e <= NL && (e - 12) % NG == 4 % NG) put((e - 13) / NG + 1, 1);
        if (NF == 5 && e > 12 && e <= NL && (e - 12) % NG == 0) put((e - 12) / NG, 2);
        if (i > NL + 1) rdx((i - NL - 2) >> 1, i & 1, 1);
        if (i == NL + 3) BARL();
        SB();
    }
    rdx(NF - 1, 0, 1); rdx(NF - 1, 1, 1);
#pragma unroll
    for (int i = 0; i < 4 * NF; ++i) {
        const int mi = (i >> 1) % NF;
        acc[mi][0] = MFMA(kf[(i >= 2 * NF) * 2 + (i & 1)], xf[i & 1][mi], acc[mi][0]);
        SB();
        if (i < 2 * NF) rdx(mi, i & 1, 2);
        SB();
    }
#pragma unroll
    for (int mi = 0; mi < NF; ++mi) asm volatile("" : "+v"(acc[mi][0]));
    STAMP(0, 19);
    const bool all = hs_new(hs, hs_take<0>(kb), M_XR);
    STAMP(0, 20);
    hs_rmw(hs, ka, M_XW, own);
    int pr[4];
#pragma unroll
    for (int l = 1; l < 4; ++l) pr[l - 1] = ((ks - l) & 3) * 32768 + (l == 3 ? 4096 : 0);
    pr[3] = pr[2] - 4096;
    unsigned pl2 = pl, ph2 = ph;
    u32x4 s01 = s0 | s1;
    __amdgpu_buffer_rsrc_t ro2 = rs_out;
    asm volatile("" : "+s"(pr[0]), "+s"(pr[1]), "+s"(pr[2]), "+s"(pr[3]), "+s"(ro2));
    asm volatile("" : "+v"(pl2), "+v"(ph2), "+v"(s01));
    auto fin = [&](int pr) {
        const f32x4 a = acc[2 * pr][0], b = acc[pr < 2 ? 2 * pr + 1 : 2 * pr][0];
        auto x = __builtin_amdgcn_permlane16_swap(pk2(a[0], a[1]), pk2(b[0], b[1]), false, false);
        auto y = __builtin_amdgcn_permlane16_swap(pk2(a[2], a[3]), pk2(b[2], b[3]), false, false);
        if (pr < 2 || !(fq & 1)) st16_out<0>(ro2, ob + pr * 64u, u32x4{x[0], y[0], x[1], y[1]});
        SB();
    };
    u32x2 o5 = {0u, 0u};
    {
        const f32x4 z = {0.f, 0.f, 0.f, 0.f};
#pragma unroll
        for (int h = 0; h < 2; ++h) {
            const f32x4 a = acc[2 * h][0], b = acc[2 * h + 1][0];
            const bf16x8 p = __builtin_bit_cast(bf16x8, u32x4{pk2(a[0], a[1]), pk2(a[2], a[3]), pk2(b[0], b[1]), pk2(b[2], b[3])});
            acc[2 * h][0] = MFMA(p, __builtin_bit_cast(bf16x8, s0), z);
            acc[2 * h + 1][0] = MFMA(p, __builtin_bit_cast(bf16x8, s1), z);
            asm volatile("" : "+v"(acc[2 * h][0]), "+v"(acc[2 * h + 1][0]));
        }
        if (NF == 5) { const f32x4 a = acc[4][0]; o5 = u32x2{pk2(a[0], a[1]), pk2(a[2], a[3])}; acc[4][0] = z; asm volatile("" : "+v"(o5), "+v"(acc[4][0])); }
    }
    __builtin_amdgcn_s_sleep(PD2 - 2);
    hs_wait2(hs, M_XW ^ 1ull << (32 + ks), M_XW ^ 1ull << (32 + ks), 0u, ka, kb);
    STAMP(1, 0);
    bf16x8 pv[2][3];
    u32x2 p5[3];
#pragma unroll
    for (int h = 0; h < (NF + 1) / 2; ++h)
#pragma unroll
        for (int l = 1; l < 4; ++l) {
            const int sl = 2 * (l - 1) + h;
            if (h < 2) pv[h][l - 1] = __builtin_bit_cast(bf16x8, __builtin_amdgcn_raw_buffer_load_b128(rs, pl2 + (sl & 3) * 1024u, pr[l - 1], 16));
            else p5[l - 1] = __builtin_amdgcn_raw_buffer_load_b64(rs, ph2 + l * 512u, pr[l == 3 ? 3 : l - 1], 16);
            SB();
        }
    if (!all) hs_add(hs, 1ull << (32 + ks));
#pragma unroll
    for (int h = 0; h < (NF + 1) / 2; ++h) {
        if (h < 2) {
#pragma unroll
            for (int l = 0; l < 3; ++l) {
                acc[2 * h][0] = MFMA(pv[h][l], __builtin_bit_cast(bf16x8, s0), acc[2 * h][0]);
                acc[2 * h + 1][0] = MFMA(pv[h][l], __builtin_bit_cast(bf16x8, s1), acc[2 * h + 1][0]);
                SB();
                if (h == 1 && l == 1 && NF == 5) fin(0);
            }
        } else {
            acc[4][0] = MFMA(__builtin_bit_cast(bf16x8, (u32x4{p5[0][0], p5[0][1], p5[1][0], p5[1][1]})), __builtin_bit_cast(bf16x8, s01), acc[4][0]);
            acc[4][0] = MFMA(__builtin_bit_cast(bf16x8, (u32x4{p5[2][0], p5[2][1], o5[0], o5[1]})), __builtin_bit_cast(bf16x8, s01), acc[4][0]);
            SB();
        }
    }
    STAMP(1, 1);
    asm volatile("" ::"v"(ka), "v"(kb), "v"(acc[NF - 1][0]));
    if (NF == 4) {
        u32x4 v[2], p0, p1;
#pragma unroll
        for (int pr = 0; pr < 2; ++pr) {
            const f32x4 a = acc[2 * pr][0], b = acc[2 * pr + 1][0];
            auto x = __builtin_amdgcn_permlane16_swap(pk2(a[0], a[1]), pk2(b[0], b[1]), false, false);
            auto y = __builtin_amdgcn_permlane16_swap(pk2(a[2], a[3]), pk2(b[2], b[3]), false, false);
            v[pr] = u32x4{x[0], y[0], x[1], y[1]};
        }
#pragma unroll
        for (int i = 0; i < 4; ++i) {
            p0[i] = __builtin_amdgcn_update_dpp(v[0][i], v[1][i], 0x128, 0xf, 0xc, false);
            p1[i] = __builtin_amdgcn_update_dpp(v[1][i], v[0][i], 0x128, 0xf, 0x3, false);
        }
        const unsigned o2 = ob - (unsigned)(fr & 8) * (1792 - 8);
        st16_out<16>(ro2, o2, p0);
        st16_out<16>(ro2, o2 + 8 * 1792u, p1);
    } else
    fin(1);
    if (NF == 5) fin(2);
}

constexpr int Q_ASL = 18432, Q_WR = 3 * Q_ASL, Q_WSL = 21504, GP = 1216, Q_DUMMY = 162816, W_A = Q_DUMMY - 14 * 4096, Q_TAB = 84 * GP;
__device__ __forceinline__ void qkv_reqW(char* smem, const char* wg, unsigned off, int c, int kw, int sw) {
    const char* g = wg + (long)kw * (32 * 2688);
    const char* sb = smem + Q_WR + sw * Q_WSL + c * 1792;
#pragma unroll
    for (int k = 0; k < 6; ++k) glds16m(g + (k & 1) * (8 * 2688) + (k >> 1) * 896, off, sb + (k & 1) * 896 + (k >> 1) * 7168, 0xffffffffffffffull);
}
struct QX { unsigned off; int r0, r1, r2; };
__device__ __forceinline__ QX qkv_offX(int c, int lane, int s0) {
    QX q;
    q.off = (unsigned)((lane >> 3) * 1792 + ((lane & 7) ^ (lane >> 3)) * 16);
    q.r0 = max(s0 + (c < 2 ? c - 16 : 16 * c - 32), 0) * 896;
    q.r1 = (s0 + 16 * c + 32) * 896;
    q.r2 = q.r1 + (c < 2 ? 64 : -63) * 896;
    return q;
}
__device__ __forceinline__ void qkv_reqX(char* smem, const u16* __restrict__ xA, const QX& q, int c, int kx, int sx) {
    const char* g = (const char*)xA + kx * 128;
    const char* sb = smem + sx * Q_ASL + c * 1024;
    glds16a(g + q.r0, q.off, sb);
    glds16a(g + q.r1, q.off, sb + 4096);
    glds16a(g + q.r2, q.off, sb + 2 * 4096);
    glds16a(g + q.r1 + 896, q.off, sb + 3 * 4096);
    if (c < 2) glds16a(g + q.r1 + 65 * 896, q.off, sb + 4 * 4096);
}
template <int V> struct QS {
    static constexpr int col(int i) { constexpr int t[3][6] = {{0, 1, 2, 5, 3, 4}, {2, 3, 1, 0, 9, 10}, {0, 1, 2, 2, 3, 4}}; return t[V][i]; }
    static constexpr int wof(int i) { return V == 1 ? ((col(i) + 5) / 7) * 7168 + ((col(i) + 5) % 7 - 5) * 32 : col(i) * 32; }
    static constexpr int lo(int i) { return V == 0 ? (i == 3 ? 3 : 1) : V == 1 ? (i == 2 || i == 3 ? 1 : 0) : 0; }
    static constexpr int hi(int i) { return i != 3 || V == 0 ? 9 : V == 1 ? 3 : 0; }
};
#define RS2 0
#define LB 1
#define LC 3
#define LW 4
template <int V>
__device__ __forceinline__ void qkv_mma(char* smem, int lane, int G0, u64 (&tt)[11]) {
    typedef QS<V> S;
    const int fr = lane & 15, fq = lane >> 4;
    const int u = fq & 1, ah = fq >> 1, q = (lane >> 2) & 3, p = lane & 3;
    constexpr int R0 = V == 0 ? 1 : 0;
    f32x4 acc[6][9];
    unsigned aw = Q_WR + Q_WSL + (16 * ah + 4 * u + q) * 224 + 8 * p + (V < 2 ? G0 * 32 : (G0 >> 3) * 7168 + 64);
    unsigned ax0 = fr * 128 + ((fq ^ (fr & 7)) * 16), ax1 = ax0 ^ 64;
    unsigned ai = (G0 * 4 + fq) * GP + fr * 8, ah0 = ai + (fr & 8) * 64;
#pragma unroll
    for (int i = 0; i < 6; ++i)
#pragma unroll
        for (int r = S::lo(i); r < S::hi(i); ++r) { acc[i][r] = f32x4{0.f, 0.f, 0.f, 0.f}; asm volatile("" : "+v"(acc[i][r])); }
    asm volatile("" : "+v"(aw), "+v"(ax0), "+v"(ax1), "+v"(ai));
    if (V) asm volatile("" : "+v"(ah0));
    const char *pw = smem + aw, *px0 = smem + ax0, *px1 = smem + ax1;
    bf16x8 xf[9];
    s16x4 wl[6], wh[6];
    auto rdw = [&](int i, int h) {
        if (S::hi(i) == 0) return;
        if (h) wh[i] = lds_tr(pw + S::wof(i) + 8 * 224); else wl[i] = lds_tr(pw + S::wof(i));
    };
    int cw = 0x2108, cx = 0x24, dw = 0, dx = 0;
    auto tick = [&](int kind, bool extra) {
#pragma unroll
        for (int g = 0; g < 3; ++g) {
            if (g == 2 && (kind == 1 || extra)) BARK();
            int n = 0;
#pragma unroll
            for (int r = 0; r < 9; ++r)
#pragma unroll
                for (int e = 0; e < 2; ++e) {
                    const int i = 2 * g + (e ^ (r & 1));
                    if (r < S::lo(i) || r >= S::hi(i)) { if (g == 2 && e == 1 && r >= R0) { xf[r] = lds_rd128((kind ? px0 : px1) + r * 2048); SB(); } continue; }
                    asm volatile("v_mfma_f32_16x16x32_bf16 %0, %1, %2, %0" : "+v"(acc[i][r]) : "v"(cat(wl[i], wh[i])), "v"(xf[r]));
                    SB();
                    if (g < 2 ? n < 4 : (n < 8 && !(n & 1))) { const int m = g < 2 ? n : n >> 1; rdw(2 * ((g + 1) % 3) + (m >> 1), m & 1); }
                    if (g == 2 && e == 1) xf[r] = lds_rd128((kind ? px0 : px1) + r * 2048);
                    if (g == 0) {
                        if (n == 4) { dw = cw & 1 ? -4 * Q_WSL : Q_WSL; asm volatile("" : "+s"(dw)); }
                        if (n == 6) { cw >>= 1; asm volatile("" : "+s"(cw)); }
                        if (n == 8 && kind) { dx = cx & 1 ? -2 * Q_ASL : Q_ASL; asm volatile("" : "+s"(dx)); }
                        if (n == 10 && kind) { cx >>= 1; asm volatile("" : "+s"(cx)); px0 += dx; }
                        if (n == 12 && kind) px1 += dx;
                    }
                    if (g == 1 && n == 5) pw += dw;
                    ++n;
                    SB();
                }
        }
    };
    auto last = [&]() {
        int fi[48], fw[48], nf = 0, na = 99;
        u32x2 cv[48];
        auto behind = [&](int f) {
            const int a = f - LW, b = f - LC;
            if (a >= 0 && a < nf) {
                const int r = fw[a];
                *reinterpret_cast<u32x2*>(smem + (r ? ai : ah0) + S::col(fi[a]) * 4 * GP + (r == 0 ? 0 : r < 5 ? 128 * r - 64 : 128 * r)) = cv[a];
                SB();
            }
            if (b >= 0 && b < nf) { const f32x4 d = acc[fi[b]][fw[b]]; cv[b] = u32x2{pk2(d[0], d[1]), pk2(d[2], d[3])}; asm volatile("" : "+v"(cv[b])); }
            if (a == na + LB) { SB(); BARK(); }
        };
#pragma unroll
        for (int h = 0; h < (RS2 ? 6 : 3); ++h) {
            const int g = h % 3;
            int n = 0;
            if (h == 3) na = nf - 1;
#pragma unroll
            for (int r = 0; r < 9; ++r)
#pragma unroll
                for (int e = 0; e < 2; ++e) {
                    const int i = 2 * g + e;
                    if (r < S::lo(i) || r >= S::hi(i) || (RS2 && (0x67 >> r & 1) != (h < 3))) continue;
                    asm volatile("v_mfma_f32_16x16x32_bf16 %0, %1, %2, %0" : "+v"(acc[i][r]) : "v"(cat(wl[i], wh[i])), "v"(xf[r]));
                    SB();
                    if (n < 2 && h < 2) { rdw(2 * h + 2 + n, 0); rdw(2 * h + 2 + n, 1); }
                    fi[nf] = i; fw[nf] = r; ++nf;
                    behind(nf - 1);
                    ++n;
                    SB();
                }
        }
        asm volatile("s_nop 7");
#pragma unroll
        for (int f = 0; f < LW; ++f) { behind(nf + f); SB(); }
    };
    TT(9);
    __builtin_amdgcn_s_setprio(LP);
    BARL();
    TT(0); TT(5);
#pragma unroll
    for (int r = R0; r < 9; ++r) {
        if (r - R0 < 4) { rdw((r - R0) >> 1, (r - R0) & 1); SB(); }
        xf[r] = lds_rd128(px0 + r * 2048);
        SB();
    }
#pragma unroll 1
    for (int s = 0; s < 6; ++s) { tick(0, s == 0); tick(1, false); }
    TT(1); TT(6);
    tick(0, false);
    BARK();
    TT(2);
    last();
    TT(3); TT(7);
}

struct AMk { f32x4 c0, c1; u32x4 m; };
__device__ __forceinline__ AMk attn_mask(unsigned ln, bool top) {
    AMk t;
    const int out = (int)(~((0x1feu << (ln & 15)) & (top ? ~0xffu : ~0u)) >> (4 * (ln >> 4)));
    unsigned b0[4], b1[4];
#pragma unroll
    for (int i = 0; i < 4; ++i) {
        b0[i] = (unsigned)__builtin_amdgcn_sbfe(out, i, 1); b1[i] = (unsigned)__builtin_amdgcn_sbfe(out, 16 + i, 1);
        t.c0[i] = __uint_as_float(b0[i] & 0xf149f2cau);
        t.c1[i] = __uint_as_float(b1[i] & 0xf149f2cau);
    }
#pragma unroll
    for (int i = 0; i < 2; ++i) {
        t.m[i] = ~((b0[2 * i] & 0xffffu) | (b0[2 * i + 1] & 0xffff0000u));
        t.m[2 + i] = ~((b1[2 * i] & 0xffffu) | (b1[2 * i + 1] & 0xffff0000u));
    }
    asm volatile("" : "+v"(t.c0), "+v"(t.c1), "+v"(t.m));
    return t;
}
struct AA { u32x4 a; u32x2 b; };
__device__ __forceinline__ AA attn_addr(unsigned ln, int w, int rt, int hg) {
    AA t;
    const unsigned fr = ln & 15, fq = ln >> 4, b = (w >> 1) * 16, z = (w & 1) * 576, gq = ((fq & 1) * 2 + (fq >> 1)) * GP + z, gv = (56 + (ln & 3)) * GP + z;
    t.a[0] = gq + (b + fr) * 8;
    t.a[1] = fq == 3 ? 1152 - 64 : t.a[0];
    t.a[2] = gq + (b + 16 + min(fr, 7u)) * 8;
    t.a[3] = gv + (b + (ln >> 2)) * 8;
    t.b[0] = gv + (b + 16 + min(ln >> 2, 7u)) * 8;
    t.b[1] = (unsigned)(rt * 56 + hg * 14 + fq) * 2048u + (w * 16 + fr) * 16u;
    asm volatile("" : "+v"(t.a), "+v"(t.b));
    return t;
}
#define Q5 tp(0)
#define Q5N 1
#define Q6 tp(1)
#define Q7 tp(2); tp(3)
#define Q8 tw()
#define QD 0
__device__ __forceinline__ unsigned qkv_attn(char* smem, const u16* xA, const u16* wqkv, const char* wo, char* ws, int rt, int hg, int tid, int w,
                                         Hs& hs, int kst, int ro, int co, u64 cb, int qi, WO& wq) {
    const int lane = tid & 63, s0 = rt * 128, c = w & 3;
    auto st = [&](int i) { const int k = kst + i; return k >= 7 ? k - 7 : k; };
    u16* o = (u16*)(ws + WS_O);
    AMk am;
    AA aa;
    unsigned hv2 = 0;
    u64 tt[11] = {0, 0, 0, 0, 0, 0, 0, 0, 0, 0, 0};
    if (w >= 4) {
        if (w == 4) qkv_mma<0>(smem, lane, 0, tt);
        else if (w == 5) qkv_mma<1>(smem, lane, 5, tt);
        else qkv_mma<2>(smem, lane, 7 * w - 33, tt);
        __builtin_amdgcn_s_setprio(0);
        const char* tb = smem + W_A + c * 1024 + lane * 16;
        aa.a = *reinterpret_cast<const u32x4*>(tb); aa.b = *reinterpret_cast<const u32x2*>(tb + 4096);
        tb = smem + Q_TAB + lane * 16;
        am.c0 = *reinterpret_cast<const f32x4*>(tb); am.c1 = *reinterpret_cast<const f32x4*>(tb + 1024);
        am.m = *reinterpret_cast<const u32x4*>(tb + 2048);
        BAR();
        TT(4);
    } else {
#define LP0 1
        __builtin_amdgcn_s_setprio(LP0);
        const char* wg = (const char*)wqkv + ((c >> 1) * 16 + (c & 1) * 4) * 2688 + hg * 224;
        const unsigned offW = (unsigned)(lane * 16 + (lane * 37 >> 9) * (2688 - 224));
        if (QD) __builtin_amdgcn_s_sleep(QD);
        TT(0);
#define XF 1
        if (!XF) { qkv_reqW(smem, wg, offW, c, 2 * kst, 1); SB(); }
        const QX qx = qkv_offX(c, lane, s0);
        qkv_reqX(smem, xA, qx, c, kst, 0);
        if (XF) { SB(); qkv_reqW(smem, wg, offW, c, 2 * kst, 1); }
        TT(1);
        qkv_reqW(smem, wg, offW, c, 2 * kst + 1, 2);
        TT(2);
        wait_vm<6>();
        TT(4);
        BARL();
        __builtin_amdgcn_s_setprio(0);
        STAMP(0, 1);
        unsigned hv = 0;
        if (w == 0) { hs.off = hs_off(lane, HS_O + rt * 256, 0, ro, co); hs_rmw(hs, hv, M_R14 | M_OW); }
#define XO 0
        const bool late = XO == 1 || (XO == 2 && ((hg + 1) & 2));
        if (!late) qkv_reqX(smem, xA, qx, c, st(1), 1);
        qkv_reqW(smem, wg, offW, c, 2 * st(1), 3);
        if (late) wait_vm<6>(); else wait_vm<10>();
        BARL();
        STAMP(0, 9);
        int sa = 4, sx = late ? 1 : 2;
        const unsigned tc = (unsigned)((c * 32 + (lane >> 1)) * 4096), ts = (lane & 1) * 4;
        const unsigned tb = (TQ + qi * 0x11111111u) & 0x77777777u | 0x88888888u;
        auto tp = [&](int i) { asm volatile("s_mov_b32 m0, %2\n\ts_nop 0\n\tglobal_load_lds_dword %0, %1" ::"v"(tc + ((tb >> 8 * i >> ts & 15) << 7)), "s"(cb), "i"(Q_DUMMY) : "memory"); };
#pragma unroll 1
        for (int s = 0; s < 5; ++s) {
            if (late) qkv_reqX(smem, xA, qx, c, st(s + 1), sx);
            qkv_reqW(smem, wg, offW, c, 2 * st(s + 1) + 1, sa);
            qkv_reqW(smem, wg, offW, c, 2 * st(s + 2), sa == 4 ? 0 : sa + 1);
            if (late) wait_vm<6>();
            else {
                qkv_reqX(smem, xA, qx, c, st(s + 2), sx);
                if (c < 2) wait_vm<11>(); else wait_vm<10>();
            }
            sa = sa >= 3 ? sa - 3 : sa + 2; sx = sx == 2 ? 0 : sx + 1;
            if (w == 0 && s == 3) { hs.snap = hs_take<63>(hv); hs_add(hs, M_OR); }
            BARL();
        }
        if (late) qkv_reqX(smem, xA, qx, c, st(6), 0);
        qkv_reqW(smem, wg, offW, c, 2 * st(6) + 1, 4);
        int l2 = lane;
        Q5;
        asm volatile("" : "+v"(l2));
        __builtin_assume(l2 >= 0 && l2 < 64);
        am = attn_mask(l2, rt + (w >> 1) == 0);
        aa = attn_addr(l2, w, rt, hg);
        const AA aa2 = attn_addr(l2, w + 4, rt, hg);
        unsigned wt = ((qi * 8 + w) * 49 + min(l2, 48)) * 128;
        asm volatile("" : "+v"(wt));
        auto tw = [&]() {
            asm volatile("s_mov_b32 m0, %2\n\ts_nop 0\n\tglobal_load_lds_dword %0, %1" ::"v"(wt), "s"(wo), "i"(Q_DUMMY) : "memory");
            asm volatile("s_mov_b32 m0, %2\n\ts_nop 0\n\tglobal_load_lds_dword %0, %1" ::"v"(wt), "s"(wo + 4 * 49 * 128), "i"(Q_DUMMY) : "memory");
        };
        wait_vm<Q5N>();
        BARL();
        Q6;
        {
            char* tq = smem + W_A + c * 1024 + l2 * 16;
            *reinterpret_cast<u32x4*>(tq) = aa2.a; *reinterpret_cast<u32x2*>(tq + 4096) = aa2.b;
            if (w == 3) {
                tq = smem + Q_TAB + l2 * 16;
                *reinterpret_cast<f32x4*>(tq) = am.c0; *reinterpret_cast<f32x4*>(tq + 1024) = am.c1; *reinterpret_cast<u32x4*>(tq + 2048) = am.m;
            }
        }
        BAR();
        if (w == 3) *reinterpret_cast<u32x2*>(smem + ((l2 & 3) * 7 + 4) * GP + 1152) = u32x2{0u, 0u};
        Q7;
        if (w == 0) hs_rmw(hs, hv2, M_R14);
        TT(3);
        BAR();
        Q8;
    }
    const unsigned m0[2] = {am.m[0], am.m[1]}, m1[2] = {am.m[2], am.m[3]};
    const f32x4 c0 = am.c0, c1 = am.c1;
    const float scale = 0.18898223650461361f, log2e = 1.4426950408889634f;
#define FTA(i)
    FTA(0);
    auto rd64 = [&](unsigned a, int g) { return *reinterpret_cast<const s16x4*>(smem + a + g * GP); };
    bf16x8 qf[4], k0[4], k1[4], v0[4], v1[4];
#pragma unroll
    for (int hh = 0; hh < 4; ++hh) {
        qf[hh] = cat(rd64(aa.a[0] + 64, 7 * hh), rd64(aa.a[1] + 64, 7 * hh + 4));
        k0[hh] = cat(rd64(aa.a[0], 28 + 7 * hh), rd64(aa.a[0], 32 + 7 * hh));
        k1[hh] = cat(rd64(aa.a[2], 28 + 7 * hh), rd64(aa.a[2], 32 + 7 * hh));
        SB();
    }
#pragma unroll
    for (int hh = 0; hh < 4; ++hh) {
        v0[hh] = cat(lds_tr(smem + aa.a[3] + 7 * hh * GP), lds_tr(smem + aa.b[0] + 7 * hh * GP));
        v1[hh] = cat(lds_tr(smem + aa.a[3] + (7 * hh + 4) * GP), lds_tr(smem + aa.b[0] + (7 * hh + 4) * GP));
    }
    SB();
    FTA(1);
#define AB2 1
    if (RS2 && AB2 == 0 && w < 4) { SB(); BARL(); SB(); }
    float l[2][4], mx[2];
#pragma unroll
    for (int hp = 0; hp < 2; ++hp) {
        const f32x4 a0 = MFMA(k0[2 * hp], qf[2 * hp], c0), a1 = MFMA(k1[2 * hp], qf[2 * hp], c1);
        const f32x4 b0 = MFMA(k0[2 * hp + 1], qf[2 * hp + 1], c0), b1 = MFMA(k1[2 * hp + 1], qf[2 * hp + 1], c1);
        float t[4];
#pragma unroll
        for (int i = 0; i < 4; ++i) {
            auto r = __builtin_amdgcn_permlane32_swap(__float_as_uint(vmax(a0[i], a1[i])), __float_as_uint(vmax(b0[i], b1[i])), false, false);
            t[i] = vmax(__uint_as_float(r[0]), __uint_as_float(r[1]));
        }
#pragma unroll
        for (int i = 0; i < 2; ++i) {
            unsigned a = pk2(t[2 * i], t[2 * i + 1]);
            a = pk2(__uint_as_float(a << 16) * scale, __uint_as_float(a & 0xffff0000u) * scale);
            l[hp][2 * i] = __uint_as_float(a << 16);
            l[hp][2 * i + 1] = __uint_as_float(a & 0xffff0000u);
        }
        mx[hp] = vmax(vmax3(l[hp][0], l[hp][1], l[hp][2]), l[hp][3]);
    }
    {
        auto r = __builtin_amdgcn_permlane16_swap(__float_as_uint(mx[0]), __float_as_uint(mx[1]), false, false);
        const float c = vmax(__uint_as_float(r[0]), __uint_as_float(r[1])) * -log2e;
        auto q2 = __builtin_amdgcn_permlane16_swap(__float_as_uint(c), __float_as_uint(c), false, false);
        mx[0] = __uint_as_float(q2[0]); mx[1] = __uint_as_float(q2[1]);
    }
    if (RS2 && AB2 == 1 && w < 4) { SB(); BARL(); SB(); }
    float sm[2];
#pragma unroll
    for (int hp = 0; hp < 2; ++hp) {
#pragma unroll
        for (int i = 0; i < 4; ++i) l[hp][i] = __builtin_amdgcn_exp2f(__builtin_fmaf(l[hp][i], log2e, mx[hp]));
        sm[hp] = (l[hp][0] + l[hp][1]) + (l[hp][2] + l[hp][3]);
    }
    {
        auto r = __builtin_amdgcn_permlane16_swap(__float_as_uint(sm[0]), __float_as_uint(sm[1]), false, false);
        const float c = __builtin_amdgcn_rcpf(__uint_as_float(r[0]) + __uint_as_float(r[1]));
        auto q2 = __builtin_amdgcn_permlane16_swap(__float_as_uint(c), __float_as_uint(c), false, false);
        sm[0] = __uint_as_float(q2[0]); sm[1] = __uint_as_float(q2[1]);
    }
    FTA(2);
    const f32x4 z = {0.f, 0.f, 0.f, 0.f};
    const __amdgpu_buffer_rsrc_t rs_o = __builtin_amdgcn_make_buffer_rsrc(o, 0, 2048 * 448 * 2, 0x00020000);
    const bool top = aa.a[0] != aa.a[1];
#pragma unroll
    for (int hp = 0; hp < 2; ++hp) {
        const unsigned p01 = pk2(l[hp][0] * sm[hp], l[hp][1] * sm[hp]), p23 = pk2(l[hp][2] * sm[hp], l[hp][3] * sm[hp]);
        auto e01 = __builtin_amdgcn_permlane32_swap(p01, p01, false, false);
        auto e23 = __builtin_amdgcn_permlane32_swap(p23, p23, false, false);
        u32x4 ov[2];
#pragma unroll
        for (int e = 0; e < 2; ++e) {
            const int hh = 2 * hp + e;
            u32x4 pw = {e01[e] & m0[0], e23[e] & m0[1], e01[e] & m1[0], e23[e] & m1[1]};
            const bf16x8 pf = __builtin_bit_cast(bf16x8, pw);
            const f32x4 o0 = MFMA(v0[hh], pf, z), o1 = MFMA(v1[hh], pf, z);
            ov[e] = u32x4{pk2(o0[0], o0[1]), pk2(o0[2], o0[3]), pk2(o1[0], o1[1]), pk2(o1[2], o1[3])};
        }
        ov[0][2] = top ? ov[1][0] : ov[0][2]; ov[0][3] = top ? ov[1][1] : ov[0][3];
        __builtin_amdgcn_raw_buffer_store_b128(ov[0], rs_o, aa.b[1], hp * 7 * 2048, 16);
        if (!top) __builtin_amdgcn_raw_buffer_store_b128(ov[1], rs_o, aa.b[1], (hp * 7 + 4) * 2048, 16);
    }
    FTA(4);
    if (w >= 4) {
        int l2 = lane;
        asm volatile("" : "+v"(l2));
        __builtin_assume(l2 >= 0 && l2 < 64);
        wo_init(wq, l2, c);
#pragma unroll 1
        for (int t = 0; t < WE; ++t) wo_req(wq, smem + W_A + t * 4096 + c * 1024);
    } else wait_vm<0>();
    const unsigned hv3 = hs_take<WE>(hv2);
    FTA(5);
    BARL();
    return hv3;
}
__device__ __forceinline__ void oproj(char* smem, u16* __restrict__ o, u16* __restrict__ out, int rt, int rg, int ct, int lane, unsigned ka, unsigned kb) {
    const int fr = lane & 15, fq = lane >> 4;
    const __amdgpu_buffer_rsrc_t rs_o = __builtin_amdgcn_make_buffer_rsrc(o, 0, 2048 * 448 * 2, 0x00020000);
    const unsigned ga = (unsigned)(rt * 56 + fq) * 2048u + (unsigned)(rg * 16 + fr) * 16u;
    bf16x8 fa[14];
#pragma unroll
    for (int ks = 0; ks < 14; ++ks) fa[ks] = __builtin_bit_cast(bf16x8, __builtin_amdgcn_raw_buffer_load_b128(rs_o, ga + ks % 7 * 8192u, ks / 7 * 57344, 16));
    SB();
    const int q = (lane >> 2) & 3, p = lane & 3, s = 2 * (fq & 1) + (q >> 1);
    const char* aw[4];
#pragma unroll
    for (int c = 0; c < 4; ++c) aw[c] = smem + (8 * fq + q) * 128 + ((c ^ s) * 32) + 8 * p;
    f32x4 acc[4];
#pragma unroll
    for (int c = 0; c < 4; ++c) acc[c] = f32x4{0.f, 0.f, 0.f, 0.f};
    constexpr int OA = 9;
    bf16x8 fb[14][4];
#pragma unroll
    for (int ks = 0; ks < 14 + OA; ++ks) {
        if (ks >= OA) {
#pragma unroll
            for (int c = 0; c < 4; ++c) acc[c] = MFMA(fb[ks - OA][c], fa[ks - OA], acc[c]);
        }
        if (ks < 14) {
#pragma unroll
            for (int c = 0; c < 4; ++c) fb[ks][c] = cat(lds_tr(aw[c] + ks * 4096), lds_tr(aw[c] + ks * 4096 + 512));
        }
        SB();
    }
    STAMP(1, 3);
    asm volatile("" ::"v"(ka), "v"(kb), "v"(acc[3]));
    const __amdgpu_buffer_rsrc_t rs_out = __builtin_amdgcn_make_buffer_rsrc(out, 0, 2048 * 896 * 2, 0x00020000);
    const unsigned ob = (unsigned)((rt * 128 + (rg >> 1) * 32 + 2 * (fr & 7) + (rg & 1)) * 896 + ct * 64 + (fr & 8) * 4 + (fq & 1) * 16 + (fq >> 1) * 8) * 2u;
    u32x4 v[2];
#pragma unroll
    for (int pr = 0; pr < 2; ++pr) {
        const f32x4 a = acc[2 * pr], b = acc[2 * pr + 1];
        auto x = __builtin_amdgcn_permlane16_swap(pk2(a[0], a[1]), pk2(b[0], b[1]), false, false);
        auto y = __builtin_amdgcn_permlane16_swap(pk2(a[2], a[3]), pk2(b[2], b[3]), false, false);
        v[pr] = u32x4{x[0], y[0], x[1], y[1]};
    }
    u32x4 p0, p1;
#pragma unroll
    for (int i = 0; i < 4; ++i) {
        p0[i] = __builtin_amdgcn_update_dpp(v[0][i], v[1][i], 0x128, 0xf, 0xc, false);
        p1[i] = __builtin_amdgcn_update_dpp(v[1][i], v[0][i], 0x128, 0xf, 0x3, false);
    }
    st16_out<16>(rs_out, ob, p0);
    st16_out<16>(rs_out, ob + 16 * 1792u, p1);
}

__global__ __launch_bounds__(512, 2) void kf(const u16* xA, const u16* wqkv, const char* xB, const char* Hr, const char* Hi, const char* Cr,
                                          const char* Ci, const char* wo, u16* out, char* ws) {
    __shared__ __attribute__((aligned(16))) char smem[LDS_TOTAL];
    int tid = threadIdx.x, bid = blockIdx.x;
    u64 pc0;
    {
        unsigned part, off;
        asm volatile("s_getpc_b64 %0\n\ts_bfe_u32 %1, %2, 0x30003\n\ts_mulk_i32 %1, %3" : "=&s"(pc0), "=&s"(part), "+s"(bid) : "i"(CODE_PART) : "scc");
        asm volatile("s_mov_b32 m0, %4\n\tv_mad_u32_u24 %0, %1, 12, %2\n\tglobal_load_lds_dword %0, %3" : "=&v"(off), "+v"(tid) : "s"(part), "s"(pc0), "i"(Q_DUMMY) : "memory");
        asm volatile("" : "+s"(bid));
    }
    __builtin_assume(tid >= 0 && tid < 512 && bid >= 0 && bid < 256);
    const int lane = tid & 63, xcd = bid & 7;
    int w = __builtin_amdgcn_readfirstlane(tid >> 6), j = bid >> 3;
    STAMP(0, 0);
    const int ks = xcd & 3, rl = j & 3, c6 = j >> 2, r8 = (xcd >> 2) * 4 + rl;
    BL q;
    if (j < 24 && w < 4) {
        unsigned ox = (unsigned)(((ks & 1) * 1024 + r8 * 64) * 896), oh = (unsigned)((c6 < 5 ? c6 * 80 : 384) * 896);
#define KR 1
        q.kc = c6 + KR * rl >= 7 ? c6 + KR * rl - 7 : c6 + KR * rl;
        b_off(q, c6 < 4 ? 79 : 63, lane, w);
        const char *p0 = xB, *p1 = Hr, *p2 = Hi;
        asm volatile("" : "+v"(q.offX), "+v"(q.offH[0]), "+v"(q.offH[1]), "+v"(q.offH[2]));
        asm volatile("" : "+s"(p0), "+s"(p1), "+s"(p2), "+s"(ox), "+s"(oh), "+s"(q.kc));
        q.xg = p0 + ox;
        q.hg = ((ks >> 1) ? p2 : p1) + oh;
        STAMP(0, 7);
        reqH(q, smem, w, 0); reqX(q, smem, w, 0);
        reqH(q, smem, w, 1); reqX(q, smem, w, 1);
        asm volatile("" ::"s"(xA), "s"(wqkv), "s"(Cr), "s"(Ci), "s"(wo), "s"(out), "s"(ws));
    }
    asm volatile("" : "+s"(w), "+s"(j));
    __builtin_assume(w >= 0 && w < 8 && j >= 0 && j < 32);
    if (w == 7) *reinterpret_cast<u32x2*>(smem + L_GO) = u32x2{0u, 0u};
    Hs hs;
    hs.base = (const unsigned*)(ws + WS_HS); hs.off = 0; hs.snap = 0;
    int ro = 0, rg = 0, ct = 0;
    bool duty = true;
    u64 again = 0, mr = M_R14;
    unsigned ka = 0, kb = 0;
    int wb = W_0;
    if (j < 24) {
        u16* ab = (u16*)ws;
        u16* px = (u16*)(ws + WS_PX);
        const int rbl = j & 7, cg = j >> 3, rb = (xcd >> 2) * 8 + rbl;
#define DU 1
        duty = DU ? (j & 19) != 3 : j >= 4;
        const int un = DU ? j - min(c6, 4) : j - 4, uc = un >= 14 ? un - 14 : un;
        ro = xcd * 2 + (un >= 14); ct = (uc >> 1) & 7; rg = (uc & 1) * 4 + (w & 3);
        const int g1 = ks * 3 + cg;
        if (w < 4) {
            const char* cgp = ((ks >> 1) ? Ci : Cr) + (long)rb * (128 * 4096) + (ks & 1) * 2048;
            b_load(smem, q, cgp, (const char*)ab + g1 * (16 * B_SL), (u64)(((cg == 0 ? TD0 : cg == 1 ? TD1 : TD2) + rbl * 0x11111111u) & 0x77777777u) << 32 |
                   (((cg == 0 ? TC0 : cg == 1 ? TC1 : TC2) + rbl * 0x11111111u) & 0x77777777u), ks, cg == 2,
                   wo + (duty ? ct : 0) * 128, duty, hs, lane, w, r8 * 2 + (c6 & 1));
        } else {
            const int cw = w - 4;
            asm volatile("" ::"s"(xB), "s"(Hr), "s"(Hi));
            hs.off = hs_off(lane, g1 * 256, HS_X + ((rb * 3 + cg) * 4 + cw) * 256, ro, uc & 15);
            unsigned v1 = 0;
            hs_rmw(hs, v1, w == 5 ? M_LO | M_HI | M_XW | M_XR | M_OW : M_XW | M_XR);
            STAMP(0, 7);
            auto post = [&]() {
                if (w == 5) *reinterpret_cast<u32x2*>(smem + L_KEY + lane * 8) = u32x2{hs.off, hs_take<0>(v1)};
            };
            const unsigned dbase = (unsigned)((g1 * 16 + r8 * 2) * B_SL + (c6 & 1) * (c6 < 4 ? 80 : 64) * 64);
            const unsigned sg = (ks >> 1) ? 0x80008000u : 0u;
            if (c6 < 4 && !(cw & 1)) ab_mma<3>(smem, dbase, sg, lane, cw >> 1, 0, ab, post);
            else ab_mma<2>(smem, dbase, sg, lane, cw >> 1, (cw & 1) * (c6 < 4 ? 3 : 2), ab, post);
            hs.snap = hs_take<63>(v1);
            hs_add(hs, 1ull << (36 + ks));
            const unsigned pxb = (unsigned)(rb * 3 + cg) * 131072u, ob0 = (unsigned)((rb * 128 + ks * 16) * 896 + 448 + cg * 160) * 2u;
            if (cg == 2) y_mma<4>(smem, lane, cw, px, pxb, ks, out, ob0, hs);
            else y_mma<5>(smem, lane, cw, px, pxb, ks, out, ob0, hs);
            duty = false;
        }
    } else {
        const int qi = j - 24;
#define HGM 0x3102
        const int hg = HGM >> (4 * ks) & 3, rt = (xcd >> 2) * 8 + qi;
#define XT4 0x5401
        int kst = qi + (XT4 >> (4 * hg) & 15); kst = kst >= 7 ? kst - 7 : kst;
        ro = xcd * 2 + 1; ct = 3 + (qi >> 1); rg = (qi & 1) * 4 + (w & 3); duty = w < 4;
        const int co = 6 + qi;
        WO wq;
        wq.g = wo + ct * 128;
        const unsigned hv = qkv_attn(smem, xA, wqkv, wo, ws, rt, hg, tid, w, hs, kst, ro, co,
                                     (u64)(((ks >> 1) ? Ci : Cr) + (long)((xcd >> 2) * 8 + qi) * (128 * 4096) + (ks & 1) * 2048), qi, wq);
        STAMP(0, 21);
        if (w == 0) {
            hs_rmw(hs, ka, 1ull << hg | M_OW, lane == hg);
            if (!hs_new(hs, hv, mr)) again = 1ull << hg;
        }
        if (w >= 4) {
#pragma unroll 1
            for (int t = WE; t < 14; ++t) wo_req(wq, smem + W_A + t * 4096 + (w & 3) * 1024);
            wait_vm<0>();
        }
        BARL();
        wb = W_A;
    }
    if (duty) {
        volatile LDS_AS unsigned* go = (volatile LDS_AS unsigned*)(smem + L_GO);
        if (w == 0) { if (j < 24) { hs_rmw(hs, ka, M_OW); __builtin_amdgcn_s_sleep(PD2); } hs_wait2(hs, M_OW, M_OW, 0u, ka, kb); *go = 1u; }
        else lds_wait<SL2>(L_GO);
        asm volatile("" ::: "memory");
        STAMP(1, 0);
        if (w == 0) hs_add(hs, M_OR);
        oproj(smem + wb, (u16*)(ws + WS_O), out, ro, rg, ct, lane, ka, kb);
        if (again) { hs_wait(hs, mr, mr, 0u); hs_add(hs, again); }
    }
    STAMP(1, 24);
}

extern "C" void kernel_launch(void* const* d_in, const int* in_sizes, int n_in, void* d_out, int out_size,
                              void* d_ws, size_t ws_size, hipStream_t stream) {
    kf<<<256, 512, 0, stream>>>((const u16*)d_in[0], (const u16*)d_in[1], (const char*)d_in[3], (const char*)d_in[6], (const char*)d_in[7],
                                (const char*)d_in[4], (const char*)d_in[5], (const char*)d_in[2], (u16*)d_out, (char*)d_ws);
}
```
